```python
import torch

def _softmax(x, axis=-1):
    return torch.softmax(x, dim=axis)
def _gelu(x):
    return torch.nn.functional.gelu(x, approximate="tanh")
def _silu(x):
    return torch.nn.functional.silu(x)
def _sigmoid(x):
    return torch.sigmoid(x)
def _layernorm(x, axis=-1, eps=1e-5):
    m = x.mean(dim=axis, keepdim=True)
    v = x.var(dim=axis, keepdim=True, unbiased=False)
    return (x - m) / torch.sqrt(v + eps)
def _scatter_add(tgt, idx, src, axis=0):
    return tgt.clone().index_add_(axis, idx, src)
def _segment_sum(src, seg, n):
    out = torch.zeros((n,) + src.shape[1:], dtype=src.dtype)
    return out.index_add_(0, seg, src)
def _pad(x, pw):
    flat = tuple(v for pair in reversed(pw) for v in pair)
    return torch.nn.functional.pad(x, flat)

kernel_name = 'standalone__gqa_softmax__near_ridge'

def setup_inputs():

    _w = (lambda a: (lambda t: t.to(torch.bfloat16) if t.is_floating_point() else t)(torch.as_tensor(a)))
    B, Hq, Hkv, Sq, Sk, D = 8, 32, 32, 256, 1024, 128
    return {
        "q": _w((lambda *s: torch.randn(*s, dtype=torch.bfloat16) * 0.8)(B, Hq, Sq, D)),
        "k": _w((lambda *s: torch.randn(*s, dtype=torch.bfloat16) * 0.8)(B, Hkv, Sk, D)),
        "v": _w((lambda *s: torch.randn(*s, dtype=torch.bfloat16) * 0.05)(B, Hkv, Sk, D)),
    }

def reference(q, k, v):
    B, Hq, Sq, D = q.shape
    Hkv = k.shape[1]
    rep = Hq // Hkv
    k = (lambda x, n, axis: torch.repeat_interleave(x, n, dim=axis))(k, rep, axis=1)
    v = (lambda x, n, axis: torch.repeat_interleave(x, n, dim=axis))(v, rep, axis=1)
    scale = D ** -0.5
    scores = torch.einsum("bhqd,bhkd->bhqk", q, k) * scale
    p = _softmax(scores, axis=-1)
    return torch.einsum("bhqk,bhkd->bhqd", p, v)
```

```cpp
#include <hip/hip_runtime.h>
#include <hip/hip_bf16.h>
#include <cstdio>

using bf16 = __hip_bfloat16;
constexpr int   D = 128, NW = 8, QBLK = 32, KVBLK = 64;
constexpr float SCALE = 0.088388347648318440f;
#define ATTN_THR 8.f
constexpr float THR = ATTN_THR;
#define ATTN_LAYOUT 0
constexpr int LAYOUT = ATTN_LAYOUT;
#define ATTN_SDEPTH 2
constexpr int SDEPTH = ATTN_SDEPTH;
static_assert(SDEPTH == 1 || SDEPTH == 2, "SDEPTH is 1 or 2");
#define ATTN_B 8
#define ATTN_HQ 32
#define ATTN_HKV 32
#define ATTN_S 256
#define ATTN_SKV 1024
constexpr int B = ATTN_B, HQ = ATTN_HQ, HKV = ATTN_HKV, S = ATTN_S, SKV = ATTN_SKV;
constexpr int LDQ = LAYOUT ? HQ * D : D, LDK = LAYOUT ? HKV * D : D, LDO = LDQ;
static_assert(S % (QBLK * NW) == 0 && SKV % (2 * KVBLK) == 0 && HQ % HKV == 0, "S % 256 == 0, SKV % 128 == 0 (even tile count), HQ % HKV == 0");
constexpr size_t SHM_V = KVBLK * D * 2, SHM_K = KVBLK * D * 2, SHM_ATTN = 2 * SHM_V + 2 * SHM_K + NW * 64 * 4;

using bf16x8 = __attribute__((ext_vector_type(8))) short;
using s16x4  = __attribute__((ext_vector_type(4))) short;
using f32x16 = __attribute__((ext_vector_type(16))) float;
using f32x8  = __attribute__((ext_vector_type(8))) float;
using u32x4  = __attribute__((ext_vector_type(4))) unsigned;
#define KSWZ(row, colB) ((row) * 256 + ((colB) ^ (((row) & 7) << 4)))
#define SBAR() __builtin_amdgcn_sched_barrier(0)
__device__ __forceinline__ int crow(int r, int hi) { return (r & 3) + 8 * (r >> 2) + 4 * hi; }
__device__ __forceinline__ unsigned cvtpk(float lo, float hi) {
  unsigned r; asm volatile("v_cvt_pk_bf16_f32 %0, %1, %2" : "=v"(r) : "v"(lo), "v"(hi)); return r;
}
template <typename TIn> struct Stage;
template <> struct Stage<bf16>  { using T = bf16x8;
  __device__ static __forceinline__ T ld8(const bf16* p) { return *reinterpret_cast<const bf16x8*>(p); }
  __device__ static __forceinline__ bf16x8 tobf(T x) { return x; } };
template <> struct Stage<float> { using T = f32x8;
  __device__ static __forceinline__ T ld8(const float* p) { return *reinterpret_cast<const f32x8*>(p); }
  __device__ static __forceinline__ bf16x8 tobf(T x) {
    u32x4 w = {cvtpk(x[0], x[1]), cvtpk(x[2], x[3]), cvtpk(x[4], x[5]), cvtpk(x[6], x[7])}; return *reinterpret_cast<bf16x8*>(&w); } };

__device__ __forceinline__ void partialSM(f32x16& p0, f32x16& p1, float& m_reg, float& mn, float& alpha) {
  constexpr float C = SCALE * 1.4426950408889634f;
  float pmax = p0[0]; for (int r = 1; r < 16; ++r) pmax = fmaxf(pmax, p0[r]); for (int r = 0; r < 16; ++r) pmax = fmaxf(pmax, p1[r]);
  { auto rr = __builtin_amdgcn_permlane32_swap(__float_as_uint(pmax), __float_as_uint(pmax), false, false);
    pmax = fmaxf(__uint_as_float(rr[0]), __uint_as_float(rr[1])); }
  if (__builtin_expect(__all(pmax - m_reg <= THR / SCALE), 1)) { mn = m_reg; alpha = 1.f; }
  else { mn = fmaxf(m_reg, pmax); alpha = __builtin_amdgcn_exp2f((m_reg - mn) * C); m_reg = mn; }
  float mnC = -mn * C;
  for (int r = 0; r < 16; ++r) p0[r] = fmaf(p0[r], C, mnC); for (int r = 0; r < 16; ++r) p1[r] = fmaf(p1[r], C, mnC);
  for (int r = 0; r < 16; ++r) p0[r] = __builtin_amdgcn_exp2f(p0[r]);
}
__device__ __forceinline__ void finishSM(f32x16& p0, f32x16& p1, float alpha, float& l_reg, bf16x8& pa0, bf16x8& pa1, bf16x8& pa2, bf16x8& pa3) {
  for (int r = 0; r < 16; ++r) p1[r] = __builtin_amdgcn_exp2f(p1[r]);
  float ps = 0; for (int r = 0; r < 16; ++r) ps += p0[r]; for (int r = 0; r < 16; ++r) ps += p1[r];
  { auto rr = __builtin_amdgcn_permlane32_swap(__float_as_uint(ps), __float_as_uint(ps), false, false);
    ps = __uint_as_float(rr[0]) + __uint_as_float(rr[1]); }
  l_reg = l_reg * alpha + ps;
#define PK4(P, BASE, OUT) do { unsigned a0 = cvtpk(P[BASE + 0], P[BASE + 1]), a1 = cvtpk(P[BASE + 2], P[BASE + 3]);   \
    unsigned b0 = cvtpk(P[BASE + 4], P[BASE + 5]), b1 = cvtpk(P[BASE + 6], P[BASE + 7]);                              \
    auto r0 = __builtin_amdgcn_permlane32_swap(a0, b0, false, false); auto r1 = __builtin_amdgcn_permlane32_swap(a1, b1, false, false); \
    u32x4 w = {r0[0], r1[0], r0[1], r1[1]}; OUT = *reinterpret_cast<bf16x8*>(&w); } while (0)
  PK4(p0, 0, pa0); PK4(p0, 8, pa1); PK4(p1, 0, pa2); PK4(p1, 8, pa3);
#undef PK4
}
__device__ __forceinline__ void qkt(f32x16& p0, f32x16& p1, const bf16* Ks, const bf16x8* qr, int r32, int hi) {
  p0 = f32x16{}; p1 = f32x16{};
  for (int d0 = 0; d0 < 8; ++d0) { int cb = (d0 * 16 + hi * 8) * 2;
    bf16x8 b0 = *reinterpret_cast<const bf16x8*>((const char*)Ks + KSWZ(r32, cb));
    bf16x8 b1 = *reinterpret_cast<const bf16x8*>((const char*)Ks + KSWZ(32 + r32, cb));
    p0 = __builtin_amdgcn_mfma_f32_32x32x16_bf16(b0, qr[d0], p0, 0, 0, 0);
    p1 = __builtin_amdgcn_mfma_f32_32x32x16_bf16(b1, qr[d0], p1, 0, 0, 0); }
}
__device__ __forceinline__ int v_st(int k, int c) { const int kk = (k & ~0xC) | ((k & 4) << 1) | ((k & 8) >> 1); return ((kk >> 3) * 4 + (c >> 5)) * 512 + ((kk & 7) * 32 + (c & 31)) * 2; }
__device__ __forceinline__ int v_rd_base(int lane) { return ((lane & 3) << 3) | (((lane >> 2) & 3) << 6) | (((lane >> 4) & 1) << 5) | (((lane >> 5) & 1) << 8); }
constexpr int v_rd_off(int d0, int ks, int half) { return d0 * 512 + ks * 4096 + half * 2048; }
template <int OFF> __device__ __forceinline__ s16x4 tr_read(int vb) {
  s16x4 r; asm volatile("ds_read_b64_tr_b16 %0, %1 offset:%2" : "=&v"(r) : "v"(vb), "i"(OFF) : "memory"); return r;
}
template <int D0> __device__ __forceinline__ void pv_one(f32x16& od, int vb, bf16x8 pa0, bf16x8 pa1, bf16x8 pa2, bf16x8 pa3) {
  const s16x4 l0 = tr_read<v_rd_off(D0, 0, 0)>(vb), h0 = tr_read<v_rd_off(D0, 0, 1)>(vb), l1 = tr_read<v_rd_off(D0, 1, 0)>(vb), h1 = tr_read<v_rd_off(D0, 1, 1)>(vb);
  const s16x4 l2 = tr_read<v_rd_off(D0, 2, 0)>(vb), h2 = tr_read<v_rd_off(D0, 2, 1)>(vb), l3 = tr_read<v_rd_off(D0, 3, 0)>(vb), h3 = tr_read<v_rd_off(D0, 3, 1)>(vb);
  asm volatile("s_waitcnt lgkmcnt(0)" ::: "memory"); SBAR();
#define PK(L, H) (bf16x8){L[0], L[1], L[2], L[3], H[0], H[1], H[2], H[3]}
  od = __builtin_amdgcn_mfma_f32_32x32x16_bf16(pa0, PK(l0, h0), od, 0, 0, 0);
  od = __builtin_amdgcn_mfma_f32_32x32x16_bf16(pa1, PK(l1, h1), od, 0, 0, 0);
  od = __builtin_amdgcn_mfma_f32_32x32x16_bf16(pa2, PK(l2, h2), od, 0, 0, 0);
  od = __builtin_amdgcn_mfma_f32_32x32x16_bf16(pa3, PK(l3, h3), od, 0, 0, 0);
#undef PK
}
__device__ __forceinline__ void pv_d0(f32x16* o, int vb, bf16x8 pa0, bf16x8 pa1, bf16x8 pa2, bf16x8 pa3) {
  pv_one<0>(o[0], vb, pa0, pa1, pa2, pa3); pv_one<1>(o[1], vb, pa0, pa1, pa2, pa3); pv_one<2>(o[2], vb, pa0, pa1, pa2, pa3); pv_one<3>(o[3], vb, pa0, pa1, pa2, pa3);
}

template <typename TQ>
__device__ __forceinline__ void attn_dense_body(const TQ* __restrict__ Qb, const bf16* __restrict__ Kh, const bf16* __restrict__ Vh,
                                                bf16* __restrict__ Ob, int seq, char* lds) {
  using St = Stage<bf16>; using SQ = Stage<TQ>;
  const int tid = threadIdx.x, wid = tid >> 6, lane = tid & 63, r32 = lane & 31, hi = lane >> 5;
  bf16* V_lds = (bf16*)lds; bf16* K_lds = (bf16*)(lds + 2 * SHM_V);
  float* ws = (float*)(lds + 2 * SHM_V + 2 * SHM_K) + wid * 64; float* li_l = ws; float* al_l = ws + 32;
  float m_reg = -1e30f, l_reg = 0; f32x16 o[4] = {}; bf16x8 qr[8];
  const TQ* Qw = Qb + (long)(wid * QBLK + r32) * LDQ + hi * 8;
#pragma unroll
  for (int d0 = 0; d0 < 8; ++d0) qr[d0] = SQ::tobf(SQ::ld8(Qw + d0 * 16));
  const int sr = tid >> 4, sc = (tid & 15) * 8, vst0 = v_st(sr, sc), vst1 = v_st(32 + sr, sc);
  const int vb0 = (int)(uintptr_t)V_lds + v_rd_base(lane);
  struct { typename St::T vs0, vs1, ks0, ks1; } sr_[SDEPTH];
#define SLOAD(i, k0) do { sr_[i].vs0 = St::ld8(&Vh[(long)((k0) + sr) * LDK + sc]); sr_[i].vs1 = St::ld8(&Vh[(long)((k0) + 32 + sr) * LDK + sc]); \
    sr_[i].ks0 = St::ld8(&Kh[(long)((k0) + sr) * LDK + sc]); sr_[i].ks1 = St::ld8(&Kh[(long)((k0) + 32 + sr) * LDK + sc]); } while (0)
#define SWRITE(b, i) do { *(bf16x8*)((char*)V_lds + (b) * SHM_V + vst0) = St::tobf(sr_[i].vs0);          \
    *(bf16x8*)((char*)V_lds + (b) * SHM_V + vst1) = St::tobf(sr_[i].vs1); int kc = sc * 2;               \
    *(bf16x8*)((char*)K_lds + (b) * SHM_K + KSWZ(sr, kc)) = St::tobf(sr_[i].ks0);                       \
    *(bf16x8*)((char*)K_lds + (b) * SHM_K + KSWZ(32 + sr, kc)) = St::tobf(sr_[i].ks1); } while (0)
#define SWAIT() do { if constexpr (SDEPTH == 2) asm volatile("s_waitcnt vmcnt(4)" ::: "memory"); else asm volatile("s_waitcnt vmcnt(0)" ::: "memory"); } while (0)
#define RESC(a) do { if (__any((a) < 1.f)) { if (hi == 0) al_l[r32] = (a); asm volatile("s_waitcnt lgkmcnt(0)" ::: "memory"); \
    for (int d = 0; d < 4; ++d) for (int r = 0; r < 16; ++r) o[d][r] *= al_l[crow(r, hi)]; } } while (0)
  f32x16 pA0, pA1, pB0, pB1; float mnA, mnB, alA, alB; bf16x8 pa0, pa1, pa2, pa3; const int NT = seq / KVBLK;
  constexpr int SE = 0, SO = SDEPTH - 1;
  SLOAD(SE, 0); asm volatile("s_waitcnt vmcnt(0)" ::: "memory"); SWRITE(0, SE); __syncthreads();
  qkt(pA0, pA1, K_lds, qr, r32, hi); partialSM(pA0, pA1, m_reg, mnA, alA);
  SLOAD(SO, KVBLK); if constexpr (SDEPTH == 2) { if (2 < NT) SLOAD(SE, 2 * KVBLK); }
  SWAIT(); SWRITE(1, SO); __syncthreads();
  for (int j = 1; j + 1 < NT; j += 2) {
    SBAR(); qkt(pB0, pB1, (bf16*)((char*)K_lds + SHM_K), qr, r32, hi);
    finishSM(pA0, pA1, alA, l_reg, pa0, pa1, pa2, pa3); SBAR();
    SLOAD(SO, (j + SDEPTH) * KVBLK); SBAR();
    pv_d0(o, vb0, pa0, pa1, pa2, pa3); partialSM(pB0, pB1, m_reg, mnB, alB);
    __syncthreads(); SWAIT(); SWRITE(0, SE);
    RESC(alB); __syncthreads();
    SBAR(); qkt(pA0, pA1, K_lds, qr, r32, hi);
    finishSM(pB0, pB1, alB, l_reg, pa0, pa1, pa2, pa3); SBAR();
    if (SDEPTH == 1 || j + 3 < NT) SLOAD(SE, (j + 1 + SDEPTH) * KVBLK); SBAR();
    pv_d0(o, vb0 + (int)SHM_V, pa0, pa1, pa2, pa3); partialSM(pA0, pA1, m_reg, mnA, alA);
    __syncthreads(); SWAIT(); SWRITE(1, SO);
    RESC(alA); __syncthreads();
  }
  SBAR(); qkt(pB0, pB1, (bf16*)((char*)K_lds + SHM_K), qr, r32, hi);
  finishSM(pA0, pA1, alA, l_reg, pa0, pa1, pa2, pa3); SBAR();
  pv_d0(o, vb0, pa0, pa1, pa2, pa3); partialSM(pB0, pB1, m_reg, mnB, alB);
  __syncthreads(); RESC(alB);
  finishSM(pB0, pB1, alB, l_reg, pa0, pa1, pa2, pa3); SBAR();
  pv_d0(o, vb0 + (int)SHM_V, pa0, pa1, pa2, pa3);
  if (hi == 0) li_l[r32] = l_reg; asm volatile("s_waitcnt lgkmcnt(0)" ::: "memory");
  float rli[16];
#pragma unroll
  for (int r = 0; r < 16; ++r) rli[r] = __builtin_amdgcn_rcpf(li_l[crow(r, hi)]);
  bf16* Ow = Ob + (long)(wid * QBLK) * LDO;
#pragma unroll
  for (int r = 0; r < 16; ++r) { int orow = crow(r, hi);
    for (int d0 = 0; d0 < 4; ++d0) Ow[(long)orow * LDO + d0 * 32 + r32] = __float2bfloat16(o[d0][r] * rli[r]); }
#undef SLOAD
#undef SWRITE
#undef SWAIT
#undef RESC
}

template <typename TQ>
__global__ __launch_bounds__(64 * NW, 1)
void attn_fwd_dense(const TQ* __restrict__ Q, const bf16* __restrict__ K, const bf16* __restrict__ V, bf16* __restrict__ O, int sq, int skv, int GQA) {
  extern __shared__ __attribute__((aligned(16))) char shm[];
  const long b = blockIdx.z, h = blockIdx.y, Hq = gridDim.y, Hkv = Hq / GQA, kvh = h / GQA, i0 = (long)blockIdx.x * (QBLK * NW);
  if constexpr (LAYOUT == 0) {
    const long q0 = ((b * Hq + h) * sq + i0) * D, k0 = (b * Hkv + kvh) * skv * (long)D;
    attn_dense_body<TQ>(Q + q0, K + k0, V + k0, O + q0, skv, shm);
  } else {
    const long q0 = ((b * sq + i0) * HQ + h) * (long)D, k0 = (b * skv * HKV + kvh) * (long)D;
    attn_dense_body<TQ>(Q + q0, K + k0, V + k0, O + q0, skv, shm);
  }
}

extern "C" void kernel_launch(void* const* d_in, const int* in_sizes, int n_in, void* d_out, int out_size, void* d_ws, size_t ws_size, hipStream_t stream) {
  hipFuncSetAttribute((const void*)attn_fwd_dense<bf16>, hipFuncAttributeMaxDynamicSharedMemorySize, (int)SHM_ATTN);
  hipLaunchKernelGGL(attn_fwd_dense<bf16>, dim3(S / (QBLK * NW), HQ, B), dim3(64 * NW), SHM_ATTN, stream,
                     (const bf16*)d_in[0], (const bf16*)d_in[1], (const bf16*)d_in[2], (bf16*)d_out, S, SKV, HQ / HKV);
}
```

```cpp
#include <hip/hip_runtime.h>
#include <hip/hip_bf16.h>
#include <cstdio>

using bf16 = __hip_bfloat16;
constexpr int   D = 128, NW = 8, QBLK = 32, KVBLK = 64;
constexpr float SCALE = 0.088388347648318440f;
#define ATTN_THR 8.f
constexpr float THR = ATTN_THR;
#define ATTN_LAYOUT 0
constexpr int LAYOUT = ATTN_LAYOUT;
#define ATTN_SDEPTH 2
constexpr int SDEPTH = ATTN_SDEPTH;
static_assert(SDEPTH == 1 || SDEPTH == 2, "SDEPTH is 1 or 2");
#define ATTN_B 8
#define ATTN_HQ 32
#define ATTN_HKV 32
#define ATTN_S 256
#define ATTN_SKV 1024
constexpr int B = ATTN_B, HQ = ATTN_HQ, HKV = ATTN_HKV, S = ATTN_S, SKV = ATTN_SKV;
constexpr int LDQ = LAYOUT ? HQ * D : D, LDK = LAYOUT ? HKV * D : D, LDO = LDQ;
static_assert(S % (QBLK * NW) == 0 && SKV % (2 * KVBLK) == 0 && HQ % HKV == 0, "S % 256 == 0, SKV % 128 == 0 (even tile count), HQ % HKV == 0");
constexpr size_t SHM_V = KVBLK * D * 2, SHM_K = KVBLK * D * 2, SHM_ATTN = 2 * SHM_V + 2 * SHM_K + NW * 64 * 4;

using bf16x8 = __attribute__((ext_vector_type(8))) short;
using s16x4  = __attribute__((ext_vector_type(4))) short;
using f32x16 = __attribute__((ext_vector_type(16))) float;
using f32x8  = __attribute__((ext_vector_type(8))) float;
using u32x4  = __attribute__((ext_vector_type(4))) unsigned;
#define KSWZ(row, colB) ((row) * 256 + ((colB) ^ (((row) & 7) << 4)))
#define SBAR() __builtin_amdgcn_sched_barrier(0)
__device__ __forceinline__ int crow(int r, int hi) { return (r & 3) + 8 * (r >> 2) + 4 * hi; }
__device__ __forceinline__ unsigned cvtpk(float lo, float hi) {
  unsigned r; asm volatile("v_cvt_pk_bf16_f32 %0, %1, %2" : "=v"(r) : "v"(lo), "v"(hi)); return r;
}
template <typename TIn> struct Stage;
template <> struct Stage<bf16>  { using T = bf16x8;
  __device__ static __forceinline__ T ld8(const bf16* p) { return *reinterpret_cast<const bf16x8*>(p); }
  __device__ static __forceinline__ bf16x8 tobf(T x) { return x; } };
template <> struct Stage<float> { using T = f32x8;
  __device__ static __forceinline__ T ld8(const float* p) { return *reinterpret_cast<const f32x8*>(p); }
  __device__ static __forceinline__ bf16x8 tobf(T x) {
    u32x4 w = {cvtpk(x[0], x[1]), cvtpk(x[2], x[3]), cvtpk(x[4], x[5]), cvtpk(x[6], x[7])}; return *reinterpret_cast<bf16x8*>(&w); } };

__device__ __forceinline__ void partialSM(f32x16& p0, f32x16& p1, float& m_reg, float& mn, float& alpha) {
  constexpr float C = SCALE * 1.4426950408889634f;
  float pmax = p0[0]; for (int r = 1; r < 16; ++r) pmax = fmaxf(pmax, p0[r]); for (int r = 0; r < 16; ++r) pmax = fmaxf(pmax, p1[r]);
  { auto rr = __builtin_amdgcn_permlane32_swap(__float_as_uint(pmax), __float_as_uint(pmax), false, false);
    pmax = fmaxf(__uint_as_float(rr[0]), __uint_as_float(rr[1])); }
  if (__builtin_expect(__all(pmax - m_reg <= THR / SCALE), 1)) { mn = m_reg; alpha = 1.f; }
  else { mn = fmaxf(m_reg, pmax); alpha = __builtin_amdgcn_exp2f((m_reg - mn) * C); m_reg = mn; }
  float mnC = -mn * C;
  for (int r = 0; r < 16; ++r) p0[r] = fmaf(p0[r], C, mnC); for (int r = 0; r < 16; ++r) p1[r] = fmaf(p1[r], C, mnC);
  for (int r = 0; r < 16; ++r) p0[r] = __builtin_amdgcn_exp2f(p0[r]);
}
__device__ __forceinline__ void finishSM(f32x16& p0, f32x16& p1, float alpha, float& l_reg, bf16x8& pa0, bf16x8& pa1, bf16x8& pa2, bf16x8& pa3) {
  for (int r = 0; r < 16; ++r) p1[r] = __builtin_amdgcn_exp2f(p1[r]);
  float ps = 0; for (int r = 0; r < 16; ++r) ps += p0[r]; for (int r = 0; r < 16; ++r) ps += p1[r];
  { auto rr = __builtin_amdgcn_permlane32_swap(__float_as_uint(ps), __float_as_uint(ps), false, false);
    ps = __uint_as_float(rr[0]) + __uint_as_float(rr[1]); }
  l_reg = l_reg * alpha + ps;
#define PK4(P, BASE, OUT) do { unsigned a0 = cvtpk(P[BASE + 0], P[BASE + 1]), a1 = cvtpk(P[BASE + 2], P[BASE + 3]);   \
    unsigned b0 = cvtpk(P[BASE + 4], P[BASE + 5]), b1 = cvtpk(P[BASE + 6], P[BASE + 7]);                              \
    auto r0 = __builtin_amdgcn_permlane32_swap(a0, b0, false, false); auto r1 = __builtin_amdgcn_permlane32_swap(a1, b1, false, false); \
    u32x4 w = {r0[0], r1[0], r0[1], r1[1]}; OUT = *reinterpret_cast<bf16x8*>(&w); } while (0)
  PK4(p0, 0, pa0); PK4(p0, 8, pa1); PK4(p1, 0, pa2); PK4(p1, 8, pa3);
#undef PK4
}
__device__ __forceinline__ void qkt(f32x16& p0, f32x16& p1, const bf16* Ks, const bf16x8* qr, int r32, int hi) {
  p0 = f32x16{}; p1 = f32x16{};
  for (int d0 = 0; d0 < 8; ++d0) { int cb = (d0 * 16 + hi * 8) * 2;
    bf16x8 b0 = *reinterpret_cast<const bf16x8*>((const char*)Ks + KSWZ(r32, cb));
    bf16x8 b1 = *reinterpret_cast<const bf16x8*>((const char*)Ks + KSWZ(32 + r32, cb));
    p0 = __builtin_amdgcn_mfma_f32_32x32x16_bf16(b0, qr[d0], p0, 0, 0, 0);
    p1 = __builtin_amdgcn_mfma_f32_32x32x16_bf16(b1, qr[d0], p1, 0, 0, 0); }
}
__device__ __forceinline__ int v_st(int k, int c) { const int kk = (k & ~0xC) | ((k & 4) << 1) | ((k & 8) >> 1); return ((kk >> 3) * 4 + (c >> 5)) * 512 + ((kk & 7) * 32 + (c & 31)) * 2; }
__device__ __forceinline__ int v_rd_base(int lane) { return ((lane & 3) << 3) | (((lane >> 2) & 3) << 6) | (((lane >> 4) & 1) << 5) | (((lane >> 5) & 1) << 8); }
constexpr int v_rd_off(int d0, int ks, int half) { return d0 * 512 + ks * 4096 + half * 2048; }
template <int OFF> __device__ __forceinline__ s16x4 tr_read(int vb) {
  s16x4 r; asm volatile("ds_read_b64_tr_b16 %0, %1 offset:%2" : "=&v"(r) : "v"(vb), "i"(OFF) : "memory"); return r;
}
template <int D0> __device__ __forceinline__ void pv_one(f32x16& od, int vb, bf16x8 pa0, bf16x8 pa1, bf16x8 pa2, bf16x8 pa3) {
  const s16x4 l0 = tr_read<v_rd_off(D0, 0, 0)>(vb), h0 = tr_read<v_rd_off(D0, 0, 1)>(vb), l1 = tr_read<v_rd_off(D0, 1, 0)>(vb), h1 = tr_read<v_rd_off(D0, 1, 1)>(vb);
  const s16x4 l2 = tr_read<v_rd_off(D0, 2, 0)>(vb), h2 = tr_read<v_rd_off(D0, 2, 1)>(vb), l3 = tr_read<v_rd_off(D0, 3, 0)>(vb), h3 = tr_read<v_rd_off(D0, 3, 1)>(vb);
  asm volatile("s_waitcnt lgkmcnt(0)" ::: "memory"); SBAR();
#define PK(L, H) (bf16x8){L[0], L[1], L[2], L[3], H[0], H[1], H[2], H[3]}
  od = __builtin_amdgcn_mfma_f32_32x32x16_bf16(pa0, PK(l0, h0), od, 0, 0, 0);
  od = __builtin_amdgcn_mfma_f32_32x32x16_bf16(pa1, PK(l1, h1), od, 0, 0, 0);
  od = __builtin_amdgcn_mfma_f32_32x32x16_bf16(pa2, PK(l2, h2), od, 0, 0, 0);
  od = __builtin_amdgcn_mfma_f32_32x32x16_bf16(pa3, PK(l3, h3), od, 0, 0, 0);
#undef PK
}
__device__ __forceinline__ void pv_d0(f32x16* o, int vb, bf16x8 pa0, bf16x8 pa1, bf16x8 pa2, bf16x8 pa3) {
  pv_one<0>(o[0], vb, pa0, pa1, pa2, pa3); pv_one<1>(o[1], vb, pa0, pa1, pa2, pa3); pv_one<2>(o[2], vb, pa0, pa1, pa2, pa3); pv_one<3>(o[3], vb, pa0, pa1, pa2, pa3);
}

template <typename TQ>
__device__ __forceinline__ void attn_dense_body(const TQ* __restrict__ Qb, const bf16* __restrict__ Kh, const bf16* __restrict__ Vh,
                                                bf16* __restrict__ Ob, int seq, char* lds) {
  using St = Stage<bf16>; using SQ = Stage<TQ>;
  const int tid = threadIdx.x, wid = tid >> 6, lane = tid & 63, r32 = lane & 31, hi = lane >> 5;
  bf16* V_lds = (bf16*)lds; bf16* K_lds = (bf16*)(lds + 2 * SHM_V);
  float* ws = (float*)(lds + 2 * SHM_V + 2 * SHM_K) + wid * 64; float* li_l = ws; float* al_l = ws + 32;
  float m_reg = -1e30f, l_reg = 0; f32x16 o[4] = {}; bf16x8 qr[8];
  const TQ* Qw = Qb + (long)(wid * QBLK + r32) * LDQ + hi * 8;
#pragma unroll
  for (int d0 = 0; d0 < 8; ++d0) qr[d0] = SQ::tobf(SQ::ld8(Qw + d0 * 16));
  const int sr = tid >> 4, sc = (tid & 15) * 8, vst0 = v_st(sr, sc), vst1 = v_st(32 + sr, sc);
  const int vb0 = (int)(uintptr_t)V_lds + v_rd_base(lane);
  struct { typename St::T vs0, vs1, ks0, ks1; } sr_[SDEPTH];
#define SLOAD(i, k0) do { sr_[i].vs0 = St::ld8(&Vh[(long)((k0) + sr) * LDK + sc]); sr_[i].vs1 = St::ld8(&Vh[(long)((k0) + 32 + sr) * LDK + sc]); \
    sr_[i].ks0 = St::ld8(&Kh[(long)((k0) + sr) * LDK + sc]); sr_[i].ks1 = St::ld8(&Kh[(long)((k0) + 32 + sr) * LDK + sc]); } while (0)
#define SWRITE(b, i) do { *(bf16x8*)((char*)V_lds + (b) * SHM_V + vst0) = St::tobf(sr_[i].vs0);          \
    *(bf16x8*)((char*)V_lds + (b) * SHM_V + vst1) = St::tobf(sr_[i].vs1); int kc = sc * 2;               \
    *(bf16x8*)((char*)K_lds + (b) * SHM_K + KSWZ(sr, kc)) = St::tobf(sr_[i].ks0);                       \
    *(bf16x8*)((char*)K_lds + (b) * SHM_K + KSWZ(32 + sr, kc)) = St::tobf(sr_[i].ks1); } while (0)
#define SWAIT() do { if constexpr (SDEPTH == 2) asm volatile("s_waitcnt vmcnt(4)" ::: "memory"); else asm volatile("s_waitcnt vmcnt(0)" ::: "memory"); } while (0)
#define RESC(a) do { if (__any((a) < 1.f)) { if (hi == 0) al_l[r32] = (a); asm volatile("s_waitcnt lgkmcnt(0)" ::: "memory"); \
    for (int d = 0; d < 4; ++d) for (int r = 0; r < 16; ++r) o[d][r] *= al_l[crow(r, hi)]; } } while (0)
  f32x16 pA0, pA1, pB0, pB1; float mnA, mnB, alA, alB; bf16x8 pa0, pa1, pa2, pa3; const int NT = seq / KVBLK;
  constexpr int SE = 0, SO = SDEPTH - 1;
  SLOAD(SE, 0); asm volatile("s_waitcnt vmcnt(0)" ::: "memory"); SWRITE(0, SE); __syncthreads();
  qkt(pA0, pA1, K_lds, qr, r32, hi); partialSM(pA0, pA1, m_reg, mnA, alA);
  SLOAD(SO, KVBLK); if constexpr (SDEPTH == 2) { if (2 < NT) SLOAD(SE, 2 * KVBLK); }
  SWAIT(); SWRITE(1, SO); __syncthreads();
  for (int j = 1; j + 1 < NT; j += 2) {
    SBAR(); qkt(pB0, pB1, (bf16*)((char*)K_lds + SHM_K), qr, r32, hi);
    finishSM(pA0, pA1, alA, l_reg, pa0, pa1, pa2, pa3); SBAR();
    SLOAD(SO, (j + SDEPTH) * KVBLK); SBAR();
    pv_d0(o, vb0, pa0, pa1, pa2, pa3); partialSM(pB0, pB1, m_reg, mnB, alB);
    __syncthreads(); SWAIT(); SWRITE(0, SE);
    RESC(alB); __syncthreads();
    SBAR(); qkt(pA0, pA1, K_lds, qr, r32, hi);
    finishSM(pB0, pB1, alB, l_reg, pa0, pa1, pa2, pa3); SBAR();
    if (SDEPTH == 1 || j + 3 < NT) SLOAD(SE, (j + 1 + SDEPTH) * KVBLK); SBAR();
    pv_d0(o, vb0 + (int)SHM_V, pa0, pa1, pa2, pa3); partialSM(pA0, pA1, m_reg, mnA, alA);
    __syncthreads(); SWAIT(); SWRITE(1, SO);
    RESC(alA); __syncthreads();
  }
  SBAR(); qkt(pB0, pB1, (bf16*)((char*)K_lds + SHM_K), qr, r32, hi);
  finishSM(pA0, pA1, alA, l_reg, pa0, pa1, pa2, pa3); SBAR();
  pv_d0(o, vb0, pa0, pa1, pa2, pa3); partialSM(pB0, pB1, m_reg, mnB, alB);
  __syncthreads(); RESC(alB);
  finishSM(pB0, pB1, alB, l_reg, pa0, pa1, pa2, pa3); SBAR();
  pv_d0(o, vb0 + (int)SHM_V, pa0, pa1, pa2, pa3);
  if (hi == 0) li_l[r32] = l_reg; asm volatile("s_waitcnt lgkmcnt(0)" ::: "memory");
  float rli[16];
#pragma unroll
  for (int r = 0; r < 16; ++r) rli[r] = __builtin_amdgcn_rcpf(li_l[crow(r, hi)]);
  bf16* Ow = Ob + (long)(wid * QBLK) * LDO;
#pragma unroll
  for (int r = 0; r < 16; ++r) { int orow = crow(r, hi);
    for (int d0 = 0; d0 < 4; ++d0) Ow[(long)orow * LDO + d0 * 32 + r32] = __float2bfloat16(o[d0][r] * rli[r]); }
#undef SLOAD
#undef SWRITE
#undef SWAIT
#undef RESC
}

template <typename TQ>
__global__ __launch_bounds__(64 * NW, 1)
void attn_fwd_dense(const TQ* __restrict__ Q, const bf16* __restrict__ K, const bf16* __restrict__ V, bf16* __restrict__ O, int sq, int skv, int GQA) {
  extern __shared__ __attribute__((aligned(16))) char shm[];
  const long b = blockIdx.z, h = blockIdx.y, Hq = gridDim.y, Hkv = Hq / GQA, kvh = h / GQA, i0 = (long)blockIdx.x * (QBLK * NW);
  if constexpr (LAYOUT == 0) {
    const long q0 = ((b * Hq + h) * sq + i0) * D, k0 = (b * Hkv + kvh) * skv * (long)D;
    attn_dense_body<TQ>(Q + q0, K + k0, V + k0, O + q0, skv, shm);
  } else {
    const long q0 = ((b * sq + i0) * HQ + h) * (long)D, k0 = (b * skv * HKV + kvh) * (long)D;
    attn_dense_body<TQ>(Q + q0, K + k0, V + k0, O + q0, skv, shm);
  }
}

typedef unsigned long long u64;

__device__ __forceinline__ unsigned rfl(unsigned x) { return __builtin_amdgcn_readfirstlane(x); }

template <int AUX> __device__ __forceinline__ void dma(unsigned m0v, unsigned voff, __amdgpu_buffer_rsrc_t srd, unsigned soff) {
  if constexpr (AUX == 0) asm volatile("s_mov_b32 m0, %0\n\ts_nop 0\n\tbuffer_load_dwordx4 %1, %2, %3 offen lds" :: "s"(m0v), "v"(voff), "s"(srd), "s"(soff) : "memory");
  if constexpr (AUX == 1) asm volatile("s_mov_b32 m0, %0\n\ts_nop 0\n\tbuffer_load_dwordx4 %1, %2, %3 offen nt lds" :: "s"(m0v), "v"(voff), "s"(srd), "s"(soff) : "memory");
  if constexpr (AUX == 2) asm volatile("s_mov_b32 m0, %0\n\ts_nop 0\n\tbuffer_load_dwordx4 %1, %2, %3 offen sc1 lds" :: "s"(m0v), "v"(voff), "s"(srd), "s"(soff) : "memory");
  if constexpr (AUX == 3) asm volatile("s_mov_b32 m0, %0\n\ts_nop 0\n\tbuffer_load_dwordx4 %1, %2, %3 offen sc1 nt lds" :: "s"(m0v), "v"(voff), "s"(srd), "s"(soff) : "memory");
  if constexpr (AUX == 4) asm volatile("s_mov_b32 m0, %0\n\ts_nop 0\n\tbuffer_load_dwordx4 %1, %2, %3 offen sc0 sc1 nt lds" :: "s"(m0v), "v"(voff), "s"(srd), "s"(soff) : "memory");
  if constexpr (AUX == 5) asm volatile("s_mov_b32 m0, %0\n\ts_nop 0\n\tbuffer_load_dwordx4 %1, %2, %3 offen sc0 sc1 lds" :: "s"(m0v), "v"(voff), "s"(srd), "s"(soff) : "memory");
  if constexpr (AUX == 6) asm volatile("s_mov_b32 m0, %0\n\ts_nop 0\n\tbuffer_load_dwordx4 %1, %2, %3 offen sc0 nt lds" :: "s"(m0v), "v"(voff), "s"(srd), "s"(soff) : "memory");
}
template <int N> __device__ __forceinline__ void waitvm() { asm volatile("s_waitcnt vmcnt(%0)" :: "i"(N) : "memory"); }

template <int AUX, int DEPTH, int ROT, int SAUX, int SLEEP>
__global__ __launch_bounds__(256, 1) void stream_k(const char* Q, const char* K, const char* V, char* O, u64* stamps) {
  __shared__ __attribute__((aligned(16))) char lds[163840];
  const unsigned tid = threadIdx.x, lane = tid & 63, wid = rfl(tid >> 6);
  const unsigned bid = blockIdx.x;
  u64 t0 = __builtin_amdgcn_s_memrealtime();
  __amdgpu_buffer_rsrc_t sq = __builtin_amdgcn_make_buffer_rsrc((void*)(Q + (size_t)bid * 65536), (short)0, 65536, 0x20000);
  __amdgpu_buffer_rsrc_t sk = __builtin_amdgcn_make_buffer_rsrc((void*)(K + (size_t)bid * 262144), (short)0, 262144, 0x20000);
  __amdgpu_buffer_rsrc_t sv = __builtin_amdgcn_make_buffer_rsrc((void*)(V + (size_t)bid * 262144), (short)0, 262144, 0x20000);
  const unsigned ldsb = (unsigned)(size_t)lds;
  const unsigned voff = lane * 16;
  const unsigned rot = ROT == 0 ? 0u : (ROT == 1 ? ((bid >> 3) & 15u) : ((bid * 5u + (bid >> 4)) & 15u));
  auto issue = [&](int i) {
    const unsigned slot = (unsigned)(i % 10) * 16384u + wid * 4096u;
    if (i < 4) {
      #pragma unroll
      for (int p = 0; p < 4; ++p) dma<AUX>(ldsb + slot + p * 1024u, voff, sq, (unsigned)i * 16384u + wid * 4096u + p * 1024u);
    } else {
      int isV, t;
      if (i == 4) { isV = 0; t = 0; } else if (i == 35) { isV = 1; t = 15; } else { int j = (i - 5) >> 1; if ((i - 5) & 1) { isV = 1; t = j; } else { isV = 0; t = j + 1; } }
      const unsigned tt = ((unsigned)t + rot) & 15u;
      #pragma unroll
      for (int p = 0; p < 4; ++p) dma<AUX>(ldsb + slot + p * 1024u, voff, isV ? sv : sk, tt * 16384u + wid * 4096u + p * 1024u);
    }
  };
  #pragma unroll
  for (int i = 0; i < DEPTH; ++i) issue(i);
  u64 t1 = 0;
  #pragma unroll
  for (int i = 0; i < 36; ++i) {
    if (i + DEPTH <= 36) waitvm<4 * (DEPTH - 1)>();
    else {
      switch (36 - i - 1) { case 0: waitvm<0>(); break; case 1: waitvm<4>(); break; case 2: waitvm<8>(); break; case 3: waitvm<12>(); break; case 4: waitvm<16>(); break;
        case 5: waitvm<20>(); break; case 6: waitvm<24>(); break; case 7: waitvm<28>(); break; case 8: waitvm<32>(); break; default: waitvm<36>(); break; }
    }
    asm volatile("s_barrier" ::: "memory");
    if (i == 4) t1 = __builtin_amdgcn_s_memrealtime();
    if (SLEEP > 0 && i >= 4) { for (int s = 0; s < SLEEP; ++s) asm volatile("s_sleep 2"); }
    if (SLEEP > 0) asm volatile("s_barrier" ::: "memory");
    if (i + DEPTH < 36) issue(i + DEPTH);
  }
  u64 t2 = __builtin_amdgcn_s_memrealtime();
  __amdgpu_buffer_rsrc_t so = __builtin_amdgcn_make_buffer_rsrc((void*)(O + (size_t)bid * 65536), (short)0, 65536, 0x20000);
  #pragma unroll
  for (int j = 0; j < 16; ++j) {
    u32x4 v = *(const u32x4*)(lds + wid * 16384 + j * 1024 + lane * 16);
    __builtin_amdgcn_raw_buffer_store_b128(v, so, wid * 16384 + j * 1024 + lane * 16, 0, SAUX);
  }
  u64 t3 = __builtin_amdgcn_s_memrealtime();
  asm volatile("s_waitcnt vmcnt(0)" ::: "memory");
  u64 t4 = __builtin_amdgcn_s_memrealtime();
  if (tid == 0) { stamps[bid * 8 + 0] = t0; stamps[bid * 8 + 1] = t1; stamps[bid * 8 + 2] = t2; stamps[bid * 8 + 3] = t3; stamps[bid * 8 + 4] = t4; }
}

__global__ void spanspin_k(const u64* st, int e, int mult) {
  __shared__ u64 a[256], b[256];
  int t = threadIdx.x; a[t] = st[t * 8]; b[t] = st[t * 8 + e]; __syncthreads();
  u64 mn = a[0], mx = b[0]; for (int i = 1; i < 256; ++i) { mn = a[i] < mn ? a[i] : mn; mx = b[i] > mx ? b[i] : mx; }
  u64 d = (mx - mn) * mult; if (d > 100000) d = 100000;
  u64 t0 = __builtin_amdgcn_s_memrealtime(); while (__builtin_amdgcn_s_memrealtime() - t0 < d) __builtin_amdgcn_s_sleep(1);
}

extern "C" void kernel_launch(void* const* d_in, const int* in_sizes, int n_in, void* d_out, int out_size, void* d_ws, size_t ws_size, hipStream_t stream) {
  hipFuncSetAttribute((const void*)attn_fwd_dense<bf16>, hipFuncAttributeMaxDynamicSharedMemorySize, (int)SHM_ATTN);
  hipLaunchKernelGGL((stream_k<0,4,0,0,0>), dim3(256), dim3(256), 0, stream, (const char*)d_in[0], (const char*)d_in[1], (const char*)d_in[2], (char*)d_out, (u64*)d_ws);
  spanspin_k<<<1, 256, 0, stream>>>((const u64*)d_ws, 4, 0);
  hipLaunchKernelGGL(attn_fwd_dense<bf16>, dim3(S / (QBLK * NW), HQ, B), dim3(64 * NW), SHM_ATTN, stream,
                     (const bf16*)d_in[0], (const bf16*)d_in[1], (const bf16*)d_in[2], (bf16*)d_out, S, SKV, HQ / HKV);
}
```

```cpp
#include <hip/hip_runtime.h>
#include <hip/hip_bf16.h>
#include <cstdio>

using bf16 = __hip_bfloat16;
constexpr int   D = 128, NW = 8, QBLK = 32, KVBLK = 64;
constexpr float SCALE = 0.088388347648318440f;
#define ATTN_THR 8.f
constexpr float THR = ATTN_THR;
#define ATTN_LAYOUT 0
constexpr int LAYOUT = ATTN_LAYOUT;
#define ATTN_SDEPTH 2
constexpr int SDEPTH = ATTN_SDEPTH;
static_assert(SDEPTH == 1 || SDEPTH == 2, "SDEPTH is 1 or 2");
#define ATTN_B 8
#define ATTN_HQ 32
#define ATTN_HKV 32
#define ATTN_S 256
#define ATTN_SKV 1024
constexpr int B = ATTN_B, HQ = ATTN_HQ, HKV = ATTN_HKV, S = ATTN_S, SKV = ATTN_SKV;
constexpr int LDQ = LAYOUT ? HQ * D : D, LDK = LAYOUT ? HKV * D : D, LDO = LDQ;
static_assert(S % (QBLK * NW) == 0 && SKV % (2 * KVBLK) == 0 && HQ % HKV == 0, "S % 256 == 0, SKV % 128 == 0 (even tile count), HQ % HKV == 0");
constexpr size_t SHM_V = KVBLK * D * 2, SHM_K = KVBLK * D * 2, SHM_ATTN = 2 * SHM_V + 2 * SHM_K + NW * 64 * 4;

using bf16x8 = __attribute__((ext_vector_type(8))) short;
using s16x4  = __attribute__((ext_vector_type(4))) short;
using f32x16 = __attribute__((ext_vector_type(16))) float;
using f32x8  = __attribute__((ext_vector_type(8))) float;
using u32x4  = __attribute__((ext_vector_type(4))) unsigned;
#define KSWZ(row, colB) ((row) * 256 + ((colB) ^ (((row) & 7) << 4)))
#define SBAR() __builtin_amdgcn_sched_barrier(0)
__device__ __forceinline__ int crow(int r, int hi) { return (r & 3) + 8 * (r >> 2) + 4 * hi; }
__device__ __forceinline__ unsigned cvtpk(float lo, float hi) {
  unsigned r; asm volatile("v_cvt_pk_bf16_f32 %0, %1, %2" : "=v"(r) : "v"(lo), "v"(hi)); return r;
}
template <typename TIn> struct Stage;
template <> struct Stage<bf16>  { using T = bf16x8;
  __device__ static __forceinline__ T ld8(const bf16* p) { return *reinterpret_cast<const bf16x8*>(p); }
  __device__ static __forceinline__ bf16x8 tobf(T x) { return x; } };
template <> struct Stage<float> { using T = f32x8;
  __device__ static __forceinline__ T ld8(const float* p) { return *reinterpret_cast<const f32x8*>(p); }
  __device__ static __forceinline__ bf16x8 tobf(T x) {
    u32x4 w = {cvtpk(x[0], x[1]), cvtpk(x[2], x[3]), cvtpk(x[4], x[5]), cvtpk(x[6], x[7])}; return *reinterpret_cast<bf16x8*>(&w); } };

__device__ __forceinline__ void partialSM(f32x16& p0, f32x16& p1, float& m_reg, float& mn, float& alpha) {
  constexpr float C = SCALE * 1.4426950408889634f;
  float pmax = p0[0]; for (int r = 1; r < 16; ++r) pmax = fmaxf(pmax, p0[r]); for (int r = 0; r < 16; ++r) pmax = fmaxf(pmax, p1[r]);
  { auto rr = __builtin_amdgcn_permlane32_swap(__float_as_uint(pmax), __float_as_uint(pmax), false, false);
    pmax = fmaxf(__uint_as_float(rr[0]), __uint_as_float(rr[1])); }
  if (__builtin_expect(__all(pmax - m_reg <= THR / SCALE), 1)) { mn = m_reg; alpha = 1.f; }
  else { mn = fmaxf(m_reg, pmax); alpha = __builtin_amdgcn_exp2f((m_reg - mn) * C); m_reg = mn; }
  float mnC = -mn * C;
  for (int r = 0; r < 16; ++r) p0[r] = fmaf(p0[r], C, mnC); for (int r = 0; r < 16; ++r) p1[r] = fmaf(p1[r], C, mnC);
  for (int r = 0; r < 16; ++r) p0[r] = __builtin_amdgcn_exp2f(p0[r]);
}
__device__ __forceinline__ void finishSM(f32x16& p0, f32x16& p1, float alpha, float& l_reg, bf16x8& pa0, bf16x8& pa1, bf16x8& pa2, bf16x8& pa3) {
  for (int r = 0; r < 16; ++r) p1[r] = __builtin_amdgcn_exp2f(p1[r]);
  float ps = 0; for (int r = 0; r < 16; ++r) ps += p0[r]; for (int r = 0; r < 16; ++r) ps += p1[r];
  { auto rr = __builtin_amdgcn_permlane32_swap(__float_as_uint(ps), __float_as_uint(ps), false, false);
    ps = __uint_as_float(rr[0]) + __uint_as_float(rr[1]); }
  l_reg = l_reg * alpha + ps;
#define PK4(P, BASE, OUT) do { unsigned a0 = cvtpk(P[BASE + 0], P[BASE + 1]), a1 = cvtpk(P[BASE + 2], P[BASE + 3]);   \
    unsigned b0 = cvtpk(P[BASE + 4], P[BASE + 5]), b1 = cvtpk(P[BASE + 6], P[BASE + 7]);                              \
    auto r0 = __builtin_amdgcn_permlane32_swap(a0, b0, false, false); auto r1 = __builtin_amdgcn_permlane32_swap(a1, b1, false, false); \
    u32x4 w = {r0[0], r1[0], r0[1], r1[1]}; OUT = *reinterpret_cast<bf16x8*>(&w); } while (0)
  PK4(p0, 0, pa0); PK4(p0, 8, pa1); PK4(p1, 0, pa2); PK4(p1, 8, pa3);
#undef PK4
}
__device__ __forceinline__ void qkt(f32x16& p0, f32x16& p1, const bf16* Ks, const bf16x8* qr, int r32, int hi) {
  p0 = f32x16{}; p1 = f32x16{};
  for (int d0 = 0; d0 < 8; ++d0) { int cb = (d0 * 16 + hi * 8) * 2;
    bf16x8 b0 = *reinterpret_cast<const bf16x8*>((const char*)Ks + KSWZ(r32, cb));
    bf16x8 b1 = *reinterpret_cast<const bf16x8*>((const char*)Ks + KSWZ(32 + r32, cb));
    p0 = __builtin_amdgcn_mfma_f32_32x32x16_bf16(b0, qr[d0], p0, 0, 0, 0);
    p1 = __builtin_amdgcn_mfma_f32_32x32x16_bf16(b1, qr[d0], p1, 0, 0, 0); }
}
__device__ __forceinline__ int v_st(int k, int c) { const int kk = (k & ~0xC) | ((k & 4) << 1) | ((k & 8) >> 1); return ((kk >> 3) * 4 + (c >> 5)) * 512 + ((kk & 7) * 32 + (c & 31)) * 2; }
__device__ __forceinline__ int v_rd_base(int lane) { return ((lane & 3) << 3) | (((lane >> 2) & 3) << 6) | (((lane >> 4) & 1) << 5) | (((lane >> 5) & 1) << 8); }
constexpr int v_rd_off(int d0, int ks, int half) { return d0 * 512 + ks * 4096 + half * 2048; }
template <int OFF> __device__ __forceinline__ s16x4 tr_read(int vb) {
  s16x4 r; asm volatile("ds_read_b64_tr_b16 %0, %1 offset:%2" : "=&v"(r) : "v"(vb), "i"(OFF) : "memory"); return r;
}
template <int D0> __device__ __forceinline__ void pv_one(f32x16& od, int vb, bf16x8 pa0, bf16x8 pa1, bf16x8 pa2, bf16x8 pa3) {
  const s16x4 l0 = tr_read<v_rd_off(D0, 0, 0)>(vb), h0 = tr_read<v_rd_off(D0, 0, 1)>(vb), l1 = tr_read<v_rd_off(D0, 1, 0)>(vb), h1 = tr_read<v_rd_off(D0, 1, 1)>(vb);
  const s16x4 l2 = tr_read<v_rd_off(D0, 2, 0)>(vb), h2 = tr_read<v_rd_off(D0, 2, 1)>(vb), l3 = tr_read<v_rd_off(D0, 3, 0)>(vb), h3 = tr_read<v_rd_off(D0, 3, 1)>(vb);
  asm volatile("s_waitcnt lgkmcnt(0)" ::: "memory"); SBAR();
#define PK(L, H) (bf16x8){L[0], L[1], L[2], L[3], H[0], H[1], H[2], H[3]}
  od = __builtin_amdgcn_mfma_f32_32x32x16_bf16(pa0, PK(l0, h0), od, 0, 0, 0);
  od = __builtin_amdgcn_mfma_f32_32x32x16_bf16(pa1, PK(l1, h1), od, 0, 0, 0);
  od = __builtin_amdgcn_mfma_f32_32x32x16_bf16(pa2, PK(l2, h2), od, 0, 0, 0);
  od = __builtin_amdgcn_mfma_f32_32x32x16_bf16(pa3, PK(l3, h3), od, 0, 0, 0);
#undef PK
}
__device__ __forceinline__ void pv_d0(f32x16* o, int vb, bf16x8 pa0, bf16x8 pa1, bf16x8 pa2, bf16x8 pa3) {
  pv_one<0>(o[0], vb, pa0, pa1, pa2, pa3); pv_one<1>(o[1], vb, pa0, pa1, pa2, pa3); pv_one<2>(o[2], vb, pa0, pa1, pa2, pa3); pv_one<3>(o[3], vb, pa0, pa1, pa2, pa3);
}

template <typename TQ>
__device__ __forceinline__ void attn_dense_body(const TQ* __restrict__ Qb, const bf16* __restrict__ Kh, const bf16* __restrict__ Vh,
                                                bf16* __restrict__ Ob, int seq, char* lds) {
  using St = Stage<bf16>; using SQ = Stage<TQ>;
  const int tid = threadIdx.x, wid = tid >> 6, lane = tid & 63, r32 = lane & 31, hi = lane >> 5;
  bf16* V_lds = (bf16*)lds; bf16* K_lds = (bf16*)(lds + 2 * SHM_V);
  float* ws = (float*)(lds + 2 * SHM_V + 2 * SHM_K) + wid * 64; float* li_l = ws; float* al_l = ws + 32;
  float m_reg = -1e30f, l_reg = 0; f32x16 o[4] = {}; bf16x8 qr[8];
  const TQ* Qw = Qb + (long)(wid * QBLK + r32) * LDQ + hi * 8;
#pragma unroll
  for (int d0 = 0; d0 < 8; ++d0) qr[d0] = SQ::tobf(SQ::ld8(Qw + d0 * 16));
  const int sr = tid >> 4, sc = (tid & 15) * 8, vst0 = v_st(sr, sc), vst1 = v_st(32 + sr, sc);
  const int vb0 = (int)(uintptr_t)V_lds + v_rd_base(lane);
  struct { typename St::T vs0, vs1, ks0, ks1; } sr_[SDEPTH];
#define SLOAD(i, k0) do { sr_[i].vs0 = St::ld8(&Vh[(long)((k0) + sr) * LDK + sc]); sr_[i].vs1 = St::ld8(&Vh[(long)((k0) + 32 + sr) * LDK + sc]); \
    sr_[i].ks0 = St::ld8(&Kh[(long)((k0) + sr) * LDK + sc]); sr_[i].ks1 = St::ld8(&Kh[(long)((k0) + 32 + sr) * LDK + sc]); } while (0)
#define SWRITE(b, i) do { *(bf16x8*)((char*)V_lds + (b) * SHM_V + vst0) = St::tobf(sr_[i].vs0);          \
    *(bf16x8*)((char*)V_lds + (b) * SHM_V + vst1) = St::tobf(sr_[i].vs1); int kc = sc * 2;               \
    *(bf16x8*)((char*)K_lds + (b) * SHM_K + KSWZ(sr, kc)) = St::tobf(sr_[i].ks0);                       \
    *(bf16x8*)((char*)K_lds + (b) * SHM_K + KSWZ(32 + sr, kc)) = St::tobf(sr_[i].ks1); } while (0)
#define SWAIT() do { if constexpr (SDEPTH == 2) asm volatile("s_waitcnt vmcnt(4)" ::: "memory"); else asm volatile("s_waitcnt vmcnt(0)" ::: "memory"); } while (0)
#define RESC(a) do { if (__any((a) < 1.f)) { if (hi == 0) al_l[r32] = (a); asm volatile("s_waitcnt lgkmcnt(0)" ::: "memory"); \
    for (int d = 0; d < 4; ++d) for (int r = 0; r < 16; ++r) o[d][r] *= al_l[crow(r, hi)]; } } while (0)
  f32x16 pA0, pA1, pB0, pB1; float mnA, mnB, alA, alB; bf16x8 pa0, pa1, pa2, pa3; const int NT = seq / KVBLK;
  constexpr int SE = 0, SO = SDEPTH - 1;
  SLOAD(SE, 0); asm volatile("s_waitcnt vmcnt(0)" ::: "memory"); SWRITE(0, SE); __syncthreads();
  qkt(pA0, pA1, K_lds, qr, r32, hi); partialSM(pA0, pA1, m_reg, mnA, alA);
  SLOAD(SO, KVBLK); if constexpr (SDEPTH == 2) { if (2 < NT) SLOAD(SE, 2 * KVBLK); }
  SWAIT(); SWRITE(1, SO); __syncthreads();
  for (int j = 1; j + 1 < NT; j += 2) {
    SBAR(); qkt(pB0, pB1, (bf16*)((char*)K_lds + SHM_K), qr, r32, hi);
    finishSM(pA0, pA1, alA, l_reg, pa0, pa1, pa2, pa3); SBAR();
    SLOAD(SO, (j + SDEPTH) * KVBLK); SBAR();
    pv_d0(o, vb0, pa0, pa1, pa2, pa3); partialSM(pB0, pB1, m_reg, mnB, alB);
    __syncthreads(); SWAIT(); SWRITE(0, SE);
    RESC(alB); __syncthreads();
    SBAR(); qkt(pA0, pA1, K_lds, qr, r32, hi);
    finishSM(pB0, pB1, alB, l_reg, pa0, pa1, pa2, pa3); SBAR();
    if (SDEPTH == 1 || j + 3 < NT) SLOAD(SE, (j + 1 + SDEPTH) * KVBLK); SBAR();
    pv_d0(o, vb0 + (int)SHM_V, pa0, pa1, pa2, pa3); partialSM(pA0, pA1, m_reg, mnA, alA);
    __syncthreads(); SWAIT(); SWRITE(1, SO);
    RESC(alA); __syncthreads();
  }
  SBAR(); qkt(pB0, pB1, (bf16*)((char*)K_lds + SHM_K), qr, r32, hi);
  finishSM(pA0, pA1, alA, l_reg, pa0, pa1, pa2, pa3); SBAR();
  pv_d0(o, vb0, pa0, pa1, pa2, pa3); partialSM(pB0, pB1, m_reg, mnB, alB);
  __syncthreads(); RESC(alB);
  finishSM(pB0, pB1, alB, l_reg, pa0, pa1, pa2, pa3); SBAR();
  pv_d0(o, vb0 + (int)SHM_V, pa0, pa1, pa2, pa3);
  if (hi == 0) li_l[r32] = l_reg; asm volatile("s_waitcnt lgkmcnt(0)" ::: "memory");
  float rli[16];
#pragma unroll
  for (int r = 0; r < 16; ++r) rli[r] = __builtin_amdgcn_rcpf(li_l[crow(r, hi)]);
  bf16* Ow = Ob + (long)(wid * QBLK) * LDO;
#pragma unroll
  for (int r = 0; r < 16; ++r) { int orow = crow(r, hi);
    for (int d0 = 0; d0 < 4; ++d0) Ow[(long)orow * LDO + d0 * 32 + r32] = __float2bfloat16(o[d0][r] * rli[r]); }
#undef SLOAD
#undef SWRITE
#undef SWAIT
#undef RESC
}

template <typename TQ>
__global__ __launch_bounds__(64 * NW, 1)
void attn_fwd_dense(const TQ* __restrict__ Q, const bf16* __restrict__ K, const bf16* __restrict__ V, bf16* __restrict__ O, int sq, int skv, int GQA, unsigned long long* st) {
  unsigned long long t0 = __builtin_amdgcn_s_memrealtime();
  extern __shared__ __attribute__((aligned(16))) char shm[];
  const long b = blockIdx.z, h = blockIdx.y, Hq = gridDim.y, Hkv = Hq / GQA, kvh = h / GQA, i0 = (long)blockIdx.x * (QBLK * NW);
  if constexpr (LAYOUT == 0) {
    const long q0 = ((b * Hq + h) * sq + i0) * D, k0 = (b * Hkv + kvh) * skv * (long)D;
    attn_dense_body<TQ>(Q + q0, K + k0, V + k0, O + q0, skv, shm);
    asm volatile("s_waitcnt vmcnt(0)" ::: "memory"); __syncthreads();
    unsigned long long t1 = __builtin_amdgcn_s_memrealtime();
    if (threadIdx.x == 0) { int bid = blockIdx.y + 32 * blockIdx.z; st[bid * 8] = t0; st[bid * 8 + 1] = t1; }
  } else {
    const long q0 = ((b * sq + i0) * HQ + h) * (long)D, k0 = (b * skv * HKV + kvh) * (long)D;
    attn_dense_body<TQ>(Q + q0, K + k0, V + k0, O + q0, skv, shm);
  }
}

typedef unsigned long long u64;
__global__ void spin_k(int ticks) { u64 t0 = __builtin_amdgcn_s_memrealtime(); while (__builtin_amdgcn_s_memrealtime() - t0 < (u64)ticks) __builtin_amdgcn_s_sleep(1); }
__global__ void spanspin_k(const u64* st, int mode, int mult) {
  u64 tme = __builtin_amdgcn_s_memrealtime();
  __shared__ u64 a[256], b[256];
  int t = threadIdx.x; a[t] = st[t * 8]; b[t] = st[t * 8 + 1]; __syncthreads();
  u64 mn = a[0], mx = b[0], mxs = a[0]; for (int i = 1; i < 256; ++i) { mn = a[i] < mn ? a[i] : mn; mx = b[i] > mx ? b[i] : mx; mxs = a[i] > mxs ? a[i] : mxs; }
  u64 d = (mode == 0 ? (mx - mn) : mode == 1 ? (tme - mx) : (mxs - mn)) * mult; if (d > 100000) d = 100000;
  u64 t0 = __builtin_amdgcn_s_memrealtime(); while (__builtin_amdgcn_s_memrealtime() - t0 < d) __builtin_amdgcn_s_sleep(1);
}

extern "C" void kernel_launch(void* const* d_in, const int* in_sizes, int n_in, void* d_out, int out_size, void* d_ws, size_t ws_size, hipStream_t stream) {
  hipFuncSetAttribute((const void*)attn_fwd_dense<bf16>, hipFuncAttributeMaxDynamicSharedMemorySize, (int)SHM_ATTN);
  hipLaunchKernelGGL(attn_fwd_dense<bf16>, dim3(S / (QBLK * NW), HQ, B), dim3(64 * NW), SHM_ATTN, stream,
                     (const bf16*)d_in[0], (const bf16*)d_in[1], (const bf16*)d_in[2], (bf16*)d_out, S, SKV, HQ / HKV, (u64*)d_ws);
}
```

```cpp
#include <hip/hip_runtime.h>
#include <hip/hip_bf16.h>

#define CLOB_V "v0","v1","v2","v3","v4","v5","v6","v7","v8","v9","v10","v11","v12","v13","v14","v15","v16","v17","v18","v19","v20","v21","v22","v23","v24","v25","v26","v27","v28","v29","v30","v31","v32","v33","v34","v35","v36","v37","v38","v39","v40","v41","v42","v43","v44","v45","v46","v47","v48","v49","v50","v51","v52","v53","v54","v55","v56","v57","v58","v59","v60","v61","v62","v63","v64","v65","v66","v67","v68","v69","v70","v71","v72","v73","v74","v75","v76","v77","v78","v79","v80","v81","v82","v83","v84","v85","v86","v87","v88","v89","v90","v91","v92","v93","v94","v95","v96","v97","v98","v99","v100","v101","v102","v103","v104","v105","v106","v107","v108","v109","v110","v111","v112","v113","v114","v115","v116","v117","v118","v119","v120","v121","v122","v123","v124","v125","v126","v127","v128","v129","v130","v131","v132","v133","v134","v135","v136","v137","v138","v139","v140","v141","v142","v143","v144","v145","v146","v147","v148","v149","v150","v151","v152","v153","v154","v155","v156","v157","v158","v159","v160","v161","v162","v163","v164","v165","v166","v167","v168","v169","v170","v171","v172","v173","v174","v175","v176","v177","v178","v179","v180","v181","v182","v183","v184","v185","v186","v187","v188","v189","v190","v191","v192","v193","v194","v195","v196","v197","v198","v199","v200","v201","v202","v203","v204","v205","v206","v207","v208","v209","v210","v211","v212","v213","v214","v215","v216","v217","v218","v219","v220","v221","v222","v223","v224","v225","v226","v227","v228","v229","v230","v231","v232","v233","v234","v235","v236","v237","v238","v239","v240","v241","v242","v243","v244","v245","v246","v247","v248","v249","v250","v251","v252","v253","v254","v255"
#define CLOB_A "a0","a1","a2","a3","a4","a5","a6","a7","a8","a9","a10","a11","a12","a13","a14","a15","a16","a17","a18","a19","a20","a21","a22","a23","a24","a25","a26","a27","a28","a29","a30","a31","a32","a33","a34","a35","a36","a37","a38","a39","a40","a41","a42","a43","a44","a45","a46","a47","a48","a49","a50","a51","a52","a53","a54","a55","a56","a57","a58","a59","a60","a61","a62","a63","a64","a65","a66","a67","a68","a69","a70","a71","a72","a73","a74","a75","a76","a77","a78","a79","a80","a81","a82","a83","a84","a85","a86","a87","a88","a89","a90","a91","a92","a93","a94","a95","a96","a97","a98","a99","a100","a101","a102","a103","a104","a105","a106","a107","a108","a109","a110","a111","a112","a113","a114","a115","a116","a117","a118","a119","a120","a121","a122","a123","a124","a125","a126","a127","a128","a129","a130","a131","a132","a133","a134","a135","a136","a137","a138","a139","a140","a141","a142","a143","a144","a145","a146","a147","a148","a149","a150","a151","a152","a153","a154","a155","a156","a157","a158","a159","a160","a161","a162","a163","a164","a165","a166","a167","a168","a169","a170","a171","a172","a173","a174","a175","a176","a177","a178","a179","a180","a181","a182","a183","a184","a185","a186","a187","a188","a189","a190","a191","a192","a193","a194","a195"
#define CLOB_S "s36","s37","s38","s39","s40","s41","s42","s43","s44","s45","s46","s47","s48","s49","s50","s51","s52","s53","s54","s55","s56","s57","s58","s59","s60","s61","s62","s63","s64","s65","s66","s67","s68","s69","s70","s71","s72","s73","s74","s75","s76","s77","s78","s79","s80","s81","s82","s83","s84","s85","s86","s87","s88","s89","s90","s91","s92","s93","s94","s95","s96","s97","s98"

__device__ __forceinline__ float bf_lo(unsigned x) { return __uint_as_float(x << 16); }
__device__ __forceinline__ float bf_hi(unsigned x) { return __uint_as_float(x & 0xffff0000u); }

__device__ __forceinline__ float slow_score(const unsigned* __restrict__ qr, const unsigned* __restrict__ kr) {
  float s = 0.f;
#pragma unroll 8
  for (int d = 0; d < 64; ++d) { const unsigned a = qr[d], b = kr[d]; s = fmaf(bf_lo(a), bf_lo(b), s); s = fmaf(bf_hi(a), bf_hi(b), s); }
  return s * 0.088388347648318440f;
}
__device__ __forceinline__ void slow_rows(const unsigned* __restrict__ q, const unsigned* __restrict__ k, const unsigned* __restrict__ v,
                                          unsigned* __restrict__ o, unsigned lane) {
  const unsigned row = (lane >> 4) * 64 + (lane & 15);
  const unsigned* qr = q + row * 64;
  float m = -__builtin_inff(), l = 0.f;
  for (int j = 0; j < 1024; ++j) {
    const float s = slow_score(qr, k + j * 64), mn = fmaxf(m, s);
    l = l * ((m == mn) ? 1.f : __expf(m - mn)) + ((mn == -__builtin_inff()) ? 0.f : __expf(s - mn));
    m = mn;
  }
  const float il = 1.f / l;
  for (int half = 0; half < 2; ++half) {
    float acc[64];
#pragma unroll
    for (int d = 0; d < 64; ++d) acc[d] = 0.f;
    for (int j = 0; j < 1024; ++j) {
      const float p = __expf(slow_score(qr, k + j * 64) - m) * il;
      const unsigned* vr = v + j * 64 + half * 32;
#pragma unroll
      for (int d = 0; d < 32; ++d) { const unsigned b = vr[d]; acc[2 * d] = fmaf(p, bf_lo(b), acc[2 * d]); acc[2 * d + 1] = fmaf(p, bf_hi(b), acc[2 * d + 1]); }
    }
#pragma unroll
    for (int d = 0; d < 32; ++d) {
      typedef float f32x2 __attribute__((ext_vector_type(2))); typedef __bf16 bf16x2 __attribute__((ext_vector_type(2)));
      f32x2 x = {acc[2 * d], acc[2 * d + 1]};
      o[row * 64 + half * 32 + d] = __builtin_bit_cast(unsigned, __builtin_convertvector(x, bf16x2));
    }
  }
}

__global__ __launch_bounds__(256, 1) void attn_fwd(const void* __restrict__ q, const void* __restrict__ k, const void* __restrict__ v, void* __restrict__ o) {
  __shared__ __attribute__((aligned(16))) char lds[163840];
  const unsigned bid = blockIdx.x, wid = __builtin_amdgcn_readfirstlane(threadIdx.x >> 6);
  unsigned flag;
  asm volatile(
    ".Lattn_begin:\n"
    "s_load_dwordx8 s[88:95], %[ka], 0x0\n"
    "v_mbcnt_lo_u32_b32 v254, -1, 0\n"
    "v_mbcnt_hi_u32_b32 v254, -1, v254\n"
    "s_lshr_b32 s53, %[bid], 3\n"
    "s_getpc_b64 s[64:65]\n"
    ".Lattn_pc:\n"
    "s_and_b32 s57, s53, 7\n"
    "s_lshl2_add_u32 s57, s57, %[wid]\n"
    "s_lshl_b32 s57, s57, 11\n"
    "v_lshrrev_b32 v160, 2, v254\n"
    "v_lshl_add_u32 v160, v160, 7, s57\n"
    "v_min_u32 v161, .Lattn_end-.Lattn_pc-4, v160\n"
    "global_load_dword v247, v161, s[64:65]\n"
    "s_and_b32 s57, %[bid], 7\n"
    "s_lshl_b32 s57, s57, 5\n"
    "s_add_u32 s52, s57, s53\n"
    "s_lshl_b32 s55, %[wid], 12\n"
    "v_lshrrev_b32 v243, 4, v254\n"
    "v_and_b32 v244, 15, v254\n"
    "v_xor_b32 v245, v243, v244\n"
    "v_lshlrev_b32 v245, 4, v245\n"
    "v_lshl_add_u32 v236, v243, 8, v245\n"
    "v_xor_b32 v237, 0x40, v236\n"
    "v_xor_b32 v238, 0x80, v236\n"
    "v_xor_b32 v239, 0xc0, v236\n"
    "s_lshl_b32 s72, s53, 14\n"
    "s_add_u32 s72, s72, s55\n"
    "s_and_b32 s72, s72, 0x3ffff\n"
    "s_and_b32 s58, s72, 0xffff\n"
    "s_lshl_b32 s68, %[wid], 14\n"
    "s_add_u32 s56, s68, 98304\n"
    "s_cmp_eq_u32 %[wid], 0\n"
    "s_cselect_b32 s56, 49152, s56\n"
    "s_waitcnt lgkmcnt(0)\n"
    "s_lshl_b32 s57, s52, 18\n"
    "s_add_u32 s40, s90, s57\n"
    "s_addc_u32 s41, s91, 0\n"
    "s_load_dword s96, s[40:41], 0x0\n"
    "s_lshl_b32 s57, s52, 18\n"
    "s_add_u32 s44, s92, s57\n"
    "s_addc_u32 s45, s93, 0\n"
    "s_load_dword s70, s[44:45], 0x0\n"
    "s_lshl_b32 s57, s52, 16\n"
    "s_add_u32 s36, s88, s57\n"
    "s_addc_u32 s37, s89, 0\n"
    "s_and_b32 s37, s37, 0xffff\n"
    "s_mov_b32 s38, 65536\n"
    "s_mov_b32 s39, 0x20000\n"
    "s_mov_b32 m0, s56\n"
    "s_nop 0\n"
    "buffer_load_dwordx4 v236, s[36:39], s58 offen offset:0 nt lds\n"
    "buffer_load_dwordx4 v237, s[36:39], s58 offen offset:1024 nt lds\n"
    "buffer_load_dwordx4 v238, s[36:39], s58 offen offset:2048 nt lds\n"
    "buffer_load_dwordx4 v239, s[36:39], s58 offen offset:3072 nt lds\n"
    "s_add_u32 s59, s58, 0x4000\n"
    "s_and_b32 s59, s59, 0xffff\n"
    "s_and_b32 s41, s41, 0xffff\n"
    "s_mov_b32 s42, 262144\n"
    "s_mov_b32 s43, 0x20000\n"
    "s_mov_b32 m0, s55\n"
    "s_nop 0\n"
    "buffer_load_dwordx4 v236, s[40:43], s72 offen offset:0 nt lds\n"
    "buffer_load_dwordx4 v237, s[40:43], s72 offen offset:1024 nt lds\n"
    "buffer_load_dwordx4 v238, s[40:43], s72 offen offset:2048 nt lds\n"
    "buffer_load_dwordx4 v239, s[40:43], s72 offen offset:3072 nt lds\n"
    "s_and_b32 s45, s45, 0xffff\n"
    "s_mov_b32 s46, 262144\n"
    "s_mov_b32 s47, 0x20000\n"
    "v_lshrrev_b32 v162, 1, v244\n"
    "v_xor_b32 v162, v162, v243\n"
    "v_lshlrev_b32 v162, 5, v162\n"
    "v_lshl_add_u32 v162, v243, 8, v162\n"
    "v_and_b32 v163, 1, v254\n"
    "v_lshl_add_u32 v240, v163, 4, v162\n"
    "v_xor_b32 v241, 0x80, v240\n"
    "s_add_u32 m0, s55, 65536\n"
    "s_nop 0\n"
    "buffer_load_dwordx4 v240, s[44:47], s72 offen offset:0 nt lds\n"
    "buffer_load_dwordx4 v241, s[44:47], s72 offen offset:1024 nt lds\n"
    "buffer_load_dwordx4 v240, s[44:47], s72 offen offset:2048 nt lds\n"
    "buffer_load_dwordx4 v241, s[44:47], s72 offen offset:3072 nt lds\n"
    "s_lshl_b32 s57, s52, 16\n"
    "s_add_u32 s48, s94, s57\n"
    "s_addc_u32 s49, s95, 0\n"
    "s_and_b32 s49, s49, 0xffff\n"
    "s_mov_b32 s50, 65536\n"
    "s_mov_b32 s51, 0x20000\n"
    "s_add_u32 s60, s59, 0x4000\n"
    "s_and_b32 s60, s60, 0xffff\n"
    "s_add_u32 s61, s60, 0x4000\n"
    "s_and_b32 s61, s61, 0xffff\n"
    "s_add_u32 s73, s72, 0x4000\n"
    "s_and_b32 s73, s73, 0x3ffff\n"
    "s_add_u32 s74, s73, 0x4000\n"
    "s_and_b32 s74, s74, 0x3ffff\n"
    "s_add_u32 s75, s74, 0x4000\n"
    "s_and_b32 s75, s75, 0x3ffff\n"
    "s_add_u32 s54, s75, 0x4000\n"
    "s_and_b32 s54, s54, 0x3ffff\n"
    "s_mov_b32 s97, s55\n"
    "s_mov_b32 s98, 4\n"
    "v_lshl_add_u32 v224, v244, 8, v245\n"
    "v_xor_b32 v225, 0x40, v224\n"
    "v_xor_b32 v226, 0x80, v224\n"
    "v_xor_b32 v227, 0xc0, v224\n"
    "v_lshrrev_b32 v160, 2, v254\n"
    "v_and_b32 v161, 7, v160\n"
    "v_lshlrev_b32 v160, 8, v160\n"
    "v_lshl_add_u32 v160, v161, 5, v160\n"
    "v_and_b32 v161, 3, v254\n"
    "v_lshl_add_u32 v160, v161, 3, v160\n"
    "v_add_u32 v228, 0x10000, v160\n"
    "v_xor_b32 v229, 0x20, v228\n"
    "v_xor_b32 v230, 0x40, v228\n"
    "v_xor_b32 v231, 0x60, v228\n"
    "v_xor_b32 v232, 0x80, v228\n"
    "v_xor_b32 v233, 0xa0, v228\n"
    "v_xor_b32 v234, 0xc0, v228\n"
    "v_xor_b32 v235, 0xe0, v228\n"
    "v_add_u32 v176, s56, v224\n"
    "v_add_u32 v177, s56, v225\n"
    "v_add_u32 v178, s56, v226\n"
    "v_add_u32 v179, s56, v227\n"
    "v_mov_b32 v164, 0x3f803f80\n"
    "v_and_b32 v165, 3, v254\n"
    "v_cmp_eq_u32 vcc, 0, v165\n"
    "s_nop 1\n"
    "v_cndmask_b32 v164, 0, v164, vcc\n"
    "v_accvgpr_write_b32 a128, v164\n"
    "v_accvgpr_write_b32 a129, v164\n"
    "v_accvgpr_write_b32 a130, v164\n"
    "v_accvgpr_write_b32 a131, v164\n"
    "s_mov_b32 s62, 0x3e0293ee\n"
    "s_mov_b32 s63, 0x3e0293ee\n"
    "s_mov_b32 s69, 0xffff0000\n"
    "s_add_u32 m0, s56, 4096\n"
    "s_nop 0\n"
    "buffer_load_dwordx4 v236, s[36:39], s59 offen offset:0 nt lds\n"
    "buffer_load_dwordx4 v237, s[36:39], s59 offen offset:1024 nt lds\n"
    "buffer_load_dwordx4 v238, s[36:39], s59 offen offset:2048 nt lds\n"
    "buffer_load_dwordx4 v239, s[36:39], s59 offen offset:3072 nt lds\n"
    "s_waitcnt vmcnt(12) lgkmcnt(0)\n"
    "ds_read_b128 v[96:99], v176 offset:0\n"
    "ds_read_b128 v[100:103], v177 offset:0\n"
    "ds_read_b128 v[104:107], v178 offset:0\n"
    "ds_read_b128 v[108:111], v179 offset:0\n"
    "s_waitcnt lgkmcnt(3)\n"
    "v_lshlrev_b32 v112, 16, v96\n"
    "v_and_b32 v113, s69, v96\n"
    "v_lshlrev_b32 v114, 16, v97\n"
    "v_and_b32 v115, s69, v97\n"
    "v_lshlrev_b32 v116, 16, v98\n"
    "v_and_b32 v117, s69, v98\n"
    "v_lshlrev_b32 v118, 16, v99\n"
    "v_and_b32 v119, s69, v99\n"
    "v_pk_mul_f32 v[112:113], v[112:113], s[62:63]\n"
    "v_pk_mul_f32 v[114:115], v[114:115], s[62:63]\n"
    "v_pk_mul_f32 v[116:117], v[116:117], s[62:63]\n"
    "v_pk_mul_f32 v[118:119], v[118:119], s[62:63]\n"
    "v_cvt_pk_bf16_f32 v96, v112, v113\n"
    "v_cvt_pk_bf16_f32 v97, v114, v115\n"
    "v_cvt_pk_bf16_f32 v98, v116, v117\n"
    "v_cvt_pk_bf16_f32 v99, v118, v119\n"
    "v_accvgpr_write_b32 a0, v96\n"
    "v_accvgpr_write_b32 a1, v97\n"
    "v_accvgpr_write_b32 a2, v98\n"
    "v_accvgpr_write_b32 a3, v99\n"
    "s_waitcnt lgkmcnt(2)\n"
    "v_lshlrev_b32 v120, 16, v100\n"
    "v_and_b32 v121, s69, v100\n"
    "v_lshlrev_b32 v122, 16, v101\n"
    "v_and_b32 v123, s69, v101\n"
    "v_lshlrev_b32 v124, 16, v102\n"
    "v_and_b32 v125, s69, v102\n"
    "v_lshlrev_b32 v126, 16, v103\n"
    "v_and_b32 v127, s69, v103\n"
    "v_pk_mul_f32 v[120:121], v[120:121], s[62:63]\n"
    "v_pk_mul_f32 v[122:123], v[122:123], s[62:63]\n"
    "v_pk_mul_f32 v[124:125], v[124:125], s[62:63]\n"
    "v_pk_mul_f32 v[126:127], v[126:127], s[62:63]\n"
    "v_cvt_pk_bf16_f32 v100, v120, v121\n"
    "v_cvt_pk_bf16_f32 v101, v122, v123\n"
    "v_cvt_pk_bf16_f32 v102, v124, v125\n"
    "v_cvt_pk_bf16_f32 v103, v126, v127\n"
    "v_accvgpr_write_b32 a4, v100\n"
    "v_accvgpr_write_b32 a5, v101\n"
    "v_accvgpr_write_b32 a6, v102\n"
    "v_accvgpr_write_b32 a7, v103\n"
    "s_waitcnt lgkmcnt(1)\n"
    "v_lshlrev_b32 v112, 16, v104\n"
    "v_and_b32 v113, s69, v104\n"
    "v_lshlrev_b32 v114, 16, v105\n"
    "v_and_b32 v115, s69, v105\n"
    "v_lshlrev_b32 v116, 16, v106\n"
    "v_and_b32 v117, s69, v106\n"
    "v_lshlrev_b32 v118, 16, v107\n"
    "v_and_b32 v119, s69, v107\n"
    "v_pk_mul_f32 v[112:113], v[112:113], s[62:63]\n"
    "v_pk_mul_f32 v[114:115], v[114:115], s[62:63]\n"
    "v_pk_mul_f32 v[116:117], v[116:117], s[62:63]\n"
    "v_pk_mul_f32 v[118:119], v[118:119], s[62:63]\n"
    "v_cvt_pk_bf16_f32 v104, v112, v113\n"
    "v_cvt_pk_bf16_f32 v105, v114, v115\n"
    "v_cvt_pk_bf16_f32 v106, v116, v117\n"
    "v_cvt_pk_bf16_f32 v107, v118, v119\n"
    "v_accvgpr_write_b32 a8, v104\n"
    "v_accvgpr_write_b32 a9, v105\n"
    "v_accvgpr_write_b32 a10, v106\n"
    "v_accvgpr_write_b32 a11, v107\n"
    "s_waitcnt lgkmcnt(0)\n"
    "v_lshlrev_b32 v120, 16, v108\n"
    "v_and_b32 v121, s69, v108\n"
    "v_lshlrev_b32 v122, 16, v109\n"
    "v_and_b32 v123, s69, v109\n"
    "v_lshlrev_b32 v124, 16, v110\n"
    "v_and_b32 v125, s69, v110\n"
    "v_lshlrev_b32 v126, 16, v111\n"
    "v_and_b32 v127, s69, v111\n"
    "v_pk_mul_f32 v[120:121], v[120:121], s[62:63]\n"
    "v_pk_mul_f32 v[122:123], v[122:123], s[62:63]\n"
    "v_pk_mul_f32 v[124:125], v[124:125], s[62:63]\n"
    "v_pk_mul_f32 v[126:127], v[126:127], s[62:63]\n"
    "v_cvt_pk_bf16_f32 v108, v120, v121\n"
    "v_cvt_pk_bf16_f32 v109, v122, v123\n"
    "v_cvt_pk_bf16_f32 v110, v124, v125\n"
    "v_cvt_pk_bf16_f32 v111, v126, v127\n"
    "v_accvgpr_write_b32 a12, v108\n"
    "v_accvgpr_write_b32 a13, v109\n"
    "v_accvgpr_write_b32 a14, v110\n"
    "v_accvgpr_write_b32 a15, v111\n"
    "s_add_u32 m0, s56, 8192\n"
    "s_nop 0\n"
    "buffer_load_dwordx4 v236, s[36:39], s60 offen offset:0 nt lds\n"
    "buffer_load_dwordx4 v237, s[36:39], s60 offen offset:1024 nt lds\n"
    "buffer_load_dwordx4 v238, s[36:39], s60 offen offset:2048 nt lds\n"
    "buffer_load_dwordx4 v239, s[36:39], s60 offen offset:3072 nt lds\n"
    "s_waitcnt vmcnt(12)\n"
    "s_barrier\n"
    "ds_read_b128 a[64:67], v224 offset:0\n"
    "ds_read_b128 a[80:83], v224 offset:4096\n"
    "ds_read_b128 a[132:135], v224 offset:8192\n"
    "ds_read_b128 a[148:151], v224 offset:12288\n"
    "ds_read_b128 a[68:71], v225 offset:0\n"
    "ds_read_b128 a[84:87], v225 offset:4096\n"
    "ds_read_b128 a[136:139], v225 offset:8192\n"
    "ds_read_b128 a[152:155], v225 offset:12288\n"
    "ds_read_b128 a[72:75], v226 offset:0\n"
    "ds_read_b128 a[88:91], v226 offset:4096\n"
    "ds_read_b128 a[140:143], v226 offset:8192\n"
    "ds_read_b128 a[156:159], v226 offset:12288\n"
    "ds_read_b128 a[76:79], v227 offset:0\n"
    "ds_read_b128 a[92:95], v227 offset:4096\n"
    "ds_read_b128 a[144:147], v227 offset:8192\n"
    "ds_read_b128 a[160:163], v227 offset:12288\n"
    "s_waitcnt lgkmcnt(0)\n"
    "v_mfma_f32_16x16x32_bf16 v[160:163], a[64:67], a[0:3], 0\n"
    "v_mfma_f32_16x16x32_bf16 v[164:167], a[80:83], a[0:3], 0\n"
    "v_mfma_f32_16x16x32_bf16 v[128:131], a[132:135], a[0:3], 0\n"
    "v_mfma_f32_16x16x32_bf16 v[132:135], a[148:151], a[0:3], 0\n"
    "v_mfma_f32_16x16x32_bf16 v[160:163], a[68:71], a[4:7], v[160:163]\n"
    "v_mfma_f32_16x16x32_bf16 v[164:167], a[84:87], a[4:7], v[164:167]\n"
    "v_mfma_f32_16x16x32_bf16 v[128:131], a[136:139], a[4:7], v[128:131]\n"
    "v_mfma_f32_16x16x32_bf16 v[132:135], a[152:155], a[4:7], v[132:135]\n"
    "v_mfma_f32_16x16x32_bf16 v[160:163], a[72:75], a[8:11], v[160:163]\n"
    "v_mfma_f32_16x16x32_bf16 v[164:167], a[88:91], a[8:11], v[164:167]\n"
    "v_mfma_f32_16x16x32_bf16 v[128:131], a[140:143], a[8:11], v[128:131]\n"
    "v_mfma_f32_16x16x32_bf16 v[132:135], a[156:159], a[8:11], v[132:135]\n"
    "v_mfma_f32_16x16x32_bf16 v[160:163], a[76:79], a[12:15], v[160:163]\n"
    "v_mfma_f32_16x16x32_bf16 v[164:167], a[92:95], a[12:15], v[164:167]\n"
    "v_mfma_f32_16x16x32_bf16 v[128:131], a[144:147], a[12:15], v[128:131]\n"
    "v_mfma_f32_16x16x32_bf16 v[132:135], a[160:163], a[12:15], v[132:135]\n"
    "s_nop 4\n"
    "v_max3_f32 v243, v160, v161, v162\n"
    "v_max3_f32 v244, v163, v164, v165\n"
    "v_max3_f32 v243, v243, v166, v167\n"
    "v_max3_f32 v244, v244, v128, v129\n"
    "v_max3_f32 v243, v243, v130, v131\n"
    "v_max3_f32 v244, v244, v132, v133\n"
    "v_max3_f32 v243, v243, v134, v135\n"
    "v_max_f32 v243, v243, v244\n"
    "v_mov_b32 v244, v243\n"
    "s_nop 1\n"
    "v_permlane16_swap_b32 v244, v243\n"
    "v_max_f32 v243, v244, v243\n"
    "v_mov_b32 v244, v243\n"
    "s_nop 1\n"
    "v_permlane32_swap_b32 v244, v243\n"
    "v_max_f32 v243, v244, v243\n"
    "v_add_f32 v243, 0xc1e00000, v243\n"
    "v_sub_f32 v160, v160, v243\n"
    "v_sub_f32 v161, v161, v243\n"
    "v_sub_f32 v162, v162, v243\n"
    "v_sub_f32 v163, v163, v243\n"
    "v_sub_f32 v164, v164, v243\n"
    "v_sub_f32 v165, v165, v243\n"
    "v_sub_f32 v166, v166, v243\n"
    "v_sub_f32 v167, v167, v243\n"
    "v_exp_f32 v160, v160\n"
    "v_sub_f32 v128, v128, v243\n"
    "v_exp_f32 v161, v161\n"
    "v_sub_f32 v129, v129, v243\n"
    "v_exp_f32 v162, v162\n"
    "v_sub_f32 v130, v130, v243\n"
    "v_exp_f32 v163, v163\n"
    "v_sub_f32 v131, v131, v243\n"
    "v_exp_f32 v164, v164\n"
    "v_sub_f32 v132, v132, v243\n"
    "v_exp_f32 v165, v165\n"
    "v_sub_f32 v133, v133, v243\n"
    "v_exp_f32 v166, v166\n"
    "v_sub_f32 v134, v134, v243\n"
    "v_exp_f32 v167, v167\n"
    "v_sub_f32 v135, v135, v243\n"
    "v_cvt_pk_bf16_f32 v160, v160, v161\n"
    "v_sub_f32 v192, 0, v243\n"
    "v_cvt_pk_bf16_f32 v161, v162, v163\n"
    "v_sub_f32 v193, 0, v243\n"
    "v_cvt_pk_bf16_f32 v162, v164, v165\n"
    "v_sub_f32 v194, 0, v243\n"
    "v_cvt_pk_bf16_f32 v163, v166, v167\n"
    "v_sub_f32 v195, 0, v243\n"
    "s_add_u32 m0, s56, 12288\n"
    "s_nop 0\n"
    "buffer_load_dwordx4 v236, s[36:39], s61 offen offset:0 nt lds\n"
    "buffer_load_dwordx4 v237, s[36:39], s61 offen offset:1024 nt lds\n"
    "buffer_load_dwordx4 v238, s[36:39], s61 offen offset:2048 nt lds\n"
    "buffer_load_dwordx4 v239, s[36:39], s61 offen offset:3072 nt lds\n"
    "s_waitcnt vmcnt(12)\n"
    "s_barrier\n"
    "ds_read_b64_tr_b16 a[164:165], v228 offset:0\n"
    "ds_read_b64_tr_b16 a[166:167], v228 offset:4096\n"
    "ds_read_b64_tr_b16 a[168:169], v229 offset:0\n"
    "ds_read_b64_tr_b16 a[170:171], v229 offset:4096\n"
    "ds_read_b64_tr_b16 a[172:173], v230 offset:0\n"
    "ds_read_b64_tr_b16 a[174:175], v230 offset:4096\n"
    "ds_read_b64_tr_b16 a[176:177], v231 offset:0\n"
    "ds_read_b64_tr_b16 a[178:179], v231 offset:4096\n"
    "ds_read_b64_tr_b16 a[180:181], v232 offset:0\n"
    "ds_read_b64_tr_b16 a[182:183], v232 offset:4096\n"
    "ds_read_b64_tr_b16 a[184:185], v233 offset:0\n"
    "ds_read_b64_tr_b16 a[186:187], v233 offset:4096\n"
    "ds_read_b64_tr_b16 a[188:189], v234 offset:0\n"
    "ds_read_b64_tr_b16 a[190:191], v234 offset:4096\n"
    "ds_read_b64_tr_b16 a[192:193], v235 offset:0\n"
    "ds_read_b64_tr_b16 a[194:195], v235 offset:4096\n"
    "s_waitcnt lgkmcnt(8)\n"
    "ds_read_b64_tr_b16 a[96:97], v228 offset:8192\n"
    "ds_read_b64_tr_b16 a[98:99], v228 offset:12288\n"
    "ds_read_b64_tr_b16 a[100:101], v229 offset:8192\n"
    "ds_read_b64_tr_b16 a[102:103], v229 offset:12288\n"
    "ds_read_b64_tr_b16 a[104:105], v230 offset:8192\n"
    "ds_read_b64_tr_b16 a[106:107], v230 offset:12288\n"
    "ds_read_b64_tr_b16 a[108:109], v231 offset:8192\n"
    "ds_read_b64_tr_b16 a[110:111], v231 offset:12288\n"
    "s_waitcnt lgkmcnt(8)\n"
    "ds_read_b64_tr_b16 a[112:113], v232 offset:8192\n"
    "ds_read_b64_tr_b16 a[114:115], v232 offset:12288\n"
    "ds_read_b64_tr_b16 a[116:117], v233 offset:8192\n"
    "ds_read_b64_tr_b16 a[118:119], v233 offset:12288\n"
    "ds_read_b64_tr_b16 a[120:121], v234 offset:8192\n"
    "ds_read_b64_tr_b16 a[122:123], v234 offset:12288\n"
    "ds_read_b64_tr_b16 a[124:125], v235 offset:8192\n"
    "ds_read_b64_tr_b16 a[126:127], v235 offset:12288\n"
    "v_mfma_f32_16x16x32_bf16 v[0:3], a[164:167], v[160:163], 0\n"
    "v_exp_f32 v128, v128\n"
    "v_mfma_f32_16x16x32_bf16 v[4:7], a[168:171], v[160:163], 0\n"
    "v_exp_f32 v129, v129\n"
    "v_mfma_f32_16x16x32_bf16 v[8:11], a[172:175], v[160:163], 0\n"
    "v_exp_f32 v130, v130\n"
    "v_mfma_f32_16x16x32_bf16 v[12:15], a[176:179], v[160:163], 0\n"
    "v_exp_f32 v131, v131\n"
    "v_mfma_f32_16x16x32_bf16 v[16:19], a[180:183], v[160:163], 0\n"
    "v_exp_f32 v132, v132\n"
    "v_mfma_f32_16x16x32_bf16 v[20:23], a[184:187], v[160:163], 0\n"
    "v_exp_f32 v133, v133\n"
    "v_mfma_f32_16x16x32_bf16 v[24:27], a[188:191], v[160:163], 0\n"
    "v_exp_f32 v134, v134\n"
    "v_mfma_f32_16x16x32_bf16 v[28:31], a[192:195], v[160:163], 0\n"
    "v_exp_f32 v135, v135\n"
    "v_mfma_f32_16x16x32_bf16 v[208:211], a[128:131], v[160:163], 0\n"
    "v_cvt_pk_bf16_f32 v128, v128, v129\n"
    "v_cvt_pk_bf16_f32 v129, v130, v131\n"
    "v_cvt_pk_bf16_f32 v130, v132, v133\n"
    "v_cvt_pk_bf16_f32 v131, v134, v135\n"
    "s_waitcnt lgkmcnt(0)\n"
    "s_nop 0\n"
    "v_mfma_f32_16x16x32_bf16 v[0:3], a[96:99], v[128:131], v[0:3]\n"
    "v_mfma_f32_16x16x32_bf16 v[4:7], a[100:103], v[128:131], v[4:7]\n"
    "v_mfma_f32_16x16x32_bf16 v[8:11], a[104:107], v[128:131], v[8:11]\n"
    "v_mfma_f32_16x16x32_bf16 v[12:15], a[108:111], v[128:131], v[12:15]\n"
    "v_mfma_f32_16x16x32_bf16 v[16:19], a[112:115], v[128:131], v[16:19]\n"
    "v_mfma_f32_16x16x32_bf16 v[20:23], a[116:119], v[128:131], v[20:23]\n"
    "v_mfma_f32_16x16x32_bf16 v[24:27], a[120:123], v[128:131], v[24:27]\n"
    "v_mfma_f32_16x16x32_bf16 v[28:31], a[124:127], v[128:131], v[28:31]\n"
    "v_mfma_f32_16x16x32_bf16 v[208:211], a[128:131], v[128:131], v[208:211]\n"
    "s_add_u32 m0, s55, 16384\n"
    "s_nop 0\n"
    "buffer_load_dwordx4 v236, s[40:43], s73 offen offset:0 nt lds\n"
    "buffer_load_dwordx4 v237, s[40:43], s73 offen offset:1024 nt lds\n"
    "buffer_load_dwordx4 v238, s[40:43], s73 offen offset:2048 nt lds\n"
    "buffer_load_dwordx4 v239, s[40:43], s73 offen offset:3072 nt lds\n"
    "s_waitcnt vmcnt(12)\n"
    "ds_read_b128 v[96:99], v176 offset:4096\n"
    "ds_read_b128 v[100:103], v177 offset:4096\n"
    "ds_read_b128 v[104:107], v178 offset:4096\n"
    "ds_read_b128 v[108:111], v179 offset:4096\n"
    "s_waitcnt lgkmcnt(3)\n"
    "v_lshlrev_b32 v112, 16, v96\n"
    "v_and_b32 v113, s69, v96\n"
    "v_lshlrev_b32 v114, 16, v97\n"
    "v_and_b32 v115, s69, v97\n"
    "v_lshlrev_b32 v116, 16, v98\n"
    "v_and_b32 v117, s69, v98\n"
    "v_lshlrev_b32 v118, 16, v99\n"
    "v_and_b32 v119, s69, v99\n"
    "v_pk_mul_f32 v[112:113], v[112:113], s[62:63]\n"
    "v_pk_mul_f32 v[114:115], v[114:115], s[62:63]\n"
    "v_pk_mul_f32 v[116:117], v[116:117], s[62:63]\n"
    "v_pk_mul_f32 v[118:119], v[118:119], s[62:63]\n"
    "v_cvt_pk_bf16_f32 v96, v112, v113\n"
    "v_cvt_pk_bf16_f32 v97, v114, v115\n"
    "v_cvt_pk_bf16_f32 v98, v116, v117\n"
    "v_cvt_pk_bf16_f32 v99, v118, v119\n"
    "v_accvgpr_write_b32 a16, v96\n"
    "v_accvgpr_write_b32 a17, v97\n"
    "v_accvgpr_write_b32 a18, v98\n"
    "v_accvgpr_write_b32 a19, v99\n"
    "s_waitcnt lgkmcnt(2)\n"
    "v_lshlrev_b32 v120, 16, v100\n"
    "v_and_b32 v121, s69, v100\n"
    "v_lshlrev_b32 v122, 16, v101\n"
    "v_and_b32 v123, s69, v101\n"
    "v_lshlrev_b32 v124, 16, v102\n"
    "v_and_b32 v125, s69, v102\n"
    "v_lshlrev_b32 v126, 16, v103\n"
    "v_and_b32 v127, s69, v103\n"
    "v_pk_mul_f32 v[120:121], v[120:121], s[62:63]\n"
    "v_pk_mul_f32 v[122:123], v[122:123], s[62:63]\n"
    "v_pk_mul_f32 v[124:125], v[124:125], s[62:63]\n"
    "v_pk_mul_f32 v[126:127], v[126:127], s[62:63]\n"
    "v_cvt_pk_bf16_f32 v100, v120, v121\n"
    "v_cvt_pk_bf16_f32 v101, v122, v123\n"
    "v_cvt_pk_bf16_f32 v102, v124, v125\n"
    "v_cvt_pk_bf16_f32 v103, v126, v127\n"
    "v_accvgpr_write_b32 a20, v100\n"
    "v_accvgpr_write_b32 a21, v101\n"
    "v_accvgpr_write_b32 a22, v102\n"
    "v_accvgpr_write_b32 a23, v103\n"
    "s_waitcnt lgkmcnt(1)\n"
    "v_lshlrev_b32 v112, 16, v104\n"
    "v_and_b32 v113, s69, v104\n"
    "v_lshlrev_b32 v114, 16, v105\n"
    "v_and_b32 v115, s69, v105\n"
    "v_lshlrev_b32 v116, 16, v106\n"
    "v_and_b32 v117, s69, v106\n"
    "v_lshlrev_b32 v118, 16, v107\n"
    "v_and_b32 v119, s69, v107\n"
    "v_pk_mul_f32 v[112:113], v[112:113], s[62:63]\n"
    "v_pk_mul_f32 v[114:115], v[114:115], s[62:63]\n"
    "v_pk_mul_f32 v[116:117], v[116:117], s[62:63]\n"
    "v_pk_mul_f32 v[118:119], v[118:119], s[62:63]\n"
    "v_cvt_pk_bf16_f32 v104, v112, v113\n"
    "v_cvt_pk_bf16_f32 v105, v114, v115\n"
    "v_cvt_pk_bf16_f32 v106, v116, v117\n"
    "v_cvt_pk_bf16_f32 v107, v118, v119\n"
    "v_accvgpr_write_b32 a24, v104\n"
    "v_accvgpr_write_b32 a25, v105\n"
    "v_accvgpr_write_b32 a26, v106\n"
    "v_accvgpr_write_b32 a27, v107\n"
    "s_waitcnt lgkmcnt(0)\n"
    "v_lshlrev_b32 v120, 16, v108\n"
    "v_and_b32 v121, s69, v108\n"
    "v_lshlrev_b32 v122, 16, v109\n"
    "v_and_b32 v123, s69, v109\n"
    "v_lshlrev_b32 v124, 16, v110\n"
    "v_and_b32 v125, s69, v110\n"
    "v_lshlrev_b32 v126, 16, v111\n"
    "v_and_b32 v127, s69, v111\n"
    "v_pk_mul_f32 v[120:121], v[120:121], s[62:63]\n"
    "v_pk_mul_f32 v[122:123], v[122:123], s[62:63]\n"
    "v_pk_mul_f32 v[124:125], v[124:125], s[62:63]\n"
    "v_pk_mul_f32 v[126:127], v[126:127], s[62:63]\n"
    "v_cvt_pk_bf16_f32 v108, v120, v121\n"
    "v_cvt_pk_bf16_f32 v109, v122, v123\n"
    "v_cvt_pk_bf16_f32 v110, v124, v125\n"
    "v_cvt_pk_bf16_f32 v111, v126, v127\n"
    "v_accvgpr_write_b32 a28, v108\n"
    "v_accvgpr_write_b32 a29, v109\n"
    "v_accvgpr_write_b32 a30, v110\n"
    "v_accvgpr_write_b32 a31, v111\n"
    "v_mfma_f32_16x16x32_bf16 v[160:163], a[64:67], a[16:19], 0\n"
    "v_mfma_f32_16x16x32_bf16 v[164:167], a[80:83], a[16:19], 0\n"
    "v_mfma_f32_16x16x32_bf16 v[136:139], a[132:135], a[16:19], 0\n"
    "v_mfma_f32_16x16x32_bf16 v[140:143], a[148:151], a[16:19], 0\n"
    "v_mfma_f32_16x16x32_bf16 v[160:163], a[68:71], a[20:23], v[160:163]\n"
    "v_mfma_f32_16x16x32_bf16 v[164:167], a[84:87], a[20:23], v[164:167]\n"
    "v_mfma_f32_16x16x32_bf16 v[136:139], a[136:139], a[20:23], v[136:139]\n"
    "v_mfma_f32_16x16x32_bf16 v[140:143], a[152:155], a[20:23], v[140:143]\n"
    "v_mfma_f32_16x16x32_bf16 v[160:163], a[72:75], a[24:27], v[160:163]\n"
    "v_mfma_f32_16x16x32_bf16 v[164:167], a[88:91], a[24:27], v[164:167]\n"
    "v_mfma_f32_16x16x32_bf16 v[136:139], a[140:143], a[24:27], v[136:139]\n"
    "v_mfma_f32_16x16x32_bf16 v[140:143], a[156:159], a[24:27], v[140:143]\n"
    "v_mfma_f32_16x16x32_bf16 v[160:163], a[76:79], a[28:31], v[160:163]\n"
    "v_mfma_f32_16x16x32_bf16 v[164:167], a[92:95], a[28:31], v[164:167]\n"
    "v_mfma_f32_16x16x32_bf16 v[136:139], a[144:147], a[28:31], v[136:139]\n"
    "v_mfma_f32_16x16x32_bf16 v[140:143], a[160:163], a[28:31], v[140:143]\n"
    "s_nop 4\n"
    "v_max3_f32 v243, v160, v161, v162\n"
    "v_max3_f32 v244, v163, v164, v165\n"
    "v_max3_f32 v243, v243, v166, v167\n"
    "v_max3_f32 v244, v244, v136, v137\n"
    "v_max3_f32 v243, v243, v138, v139\n"
    "v_max3_f32 v244, v244, v140, v141\n"
    "v_max3_f32 v243, v243, v142, v143\n"
    "v_max_f32 v243, v243, v244\n"
    "v_mov_b32 v244, v243\n"
    "s_nop 1\n"
    "v_permlane16_swap_b32 v244, v243\n"
    "v_max_f32 v243, v244, v243\n"
    "v_mov_b32 v244, v243\n"
    "s_nop 1\n"
    "v_permlane32_swap_b32 v244, v243\n"
    "v_max_f32 v243, v244, v243\n"
    "v_add_f32 v243, 0xc1e00000, v243\n"
    "v_sub_f32 v160, v160, v243\n"
    "v_sub_f32 v161, v161, v243\n"
    "v_sub_f32 v162, v162, v243\n"
    "v_sub_f32 v163, v163, v243\n"
    "v_sub_f32 v164, v164, v243\n"
    "v_sub_f32 v165, v165, v243\n"
    "v_sub_f32 v166, v166, v243\n"
    "v_sub_f32 v167, v167, v243\n"
    "v_exp_f32 v160, v160\n"
    "v_sub_f32 v136, v136, v243\n"
    "v_exp_f32 v161, v161\n"
    "v_sub_f32 v137, v137, v243\n"
    "v_exp_f32 v162, v162\n"
    "v_sub_f32 v138, v138, v243\n"
    "v_exp_f32 v163, v163\n"
    "v_sub_f32 v139, v139, v243\n"
    "v_exp_f32 v164, v164\n"
    "v_sub_f32 v140, v140, v243\n"
    "v_exp_f32 v165, v165\n"
    "v_sub_f32 v141, v141, v243\n"
    "v_exp_f32 v166, v166\n"
    "v_sub_f32 v142, v142, v243\n"
    "v_exp_f32 v167, v167\n"
    "v_sub_f32 v143, v143, v243\n"
    "v_cvt_pk_bf16_f32 v160, v160, v161\n"
    "v_sub_f32 v196, 0, v243\n"
    "v_cvt_pk_bf16_f32 v161, v162, v163\n"
    "v_sub_f32 v197, 0, v243\n"
    "v_cvt_pk_bf16_f32 v162, v164, v165\n"
    "v_sub_f32 v198, 0, v243\n"
    "v_cvt_pk_bf16_f32 v163, v166, v167\n"
    "v_sub_f32 v199, 0, v243\n"
    "s_nop 0\n"
    "v_mfma_f32_16x16x32_bf16 v[32:35], a[164:167], v[160:163], 0\n"
    "v_exp_f32 v136, v136\n"
    "v_mfma_f32_16x16x32_bf16 v[36:39], a[168:171], v[160:163], 0\n"
    "v_exp_f32 v137, v137\n"
    "v_mfma_f32_16x16x32_bf16 v[40:43], a[172:175], v[160:163], 0\n"
    "v_exp_f32 v138, v138\n"
    "v_mfma_f32_16x16x32_bf16 v[44:47], a[176:179], v[160:163], 0\n"
    "v_exp_f32 v139, v139\n"
    "v_mfma_f32_16x16x32_bf16 v[48:51], a[180:183], v[160:163], 0\n"
    "v_exp_f32 v140, v140\n"
    "v_mfma_f32_16x16x32_bf16 v[52:55], a[184:187], v[160:163], 0\n"
    "v_exp_f32 v141, v141\n"
    "v_mfma_f32_16x16x32_bf16 v[56:59], a[188:191], v[160:163], 0\n"
    "v_exp_f32 v142, v142\n"
    "v_mfma_f32_16x16x32_bf16 v[60:63], a[192:195], v[160:163], 0\n"
    "v_exp_f32 v143, v143\n"
    "v_mfma_f32_16x16x32_bf16 v[212:215], a[128:131], v[160:163], 0\n"
    "v_cvt_pk_bf16_f32 v136, v136, v137\n"
    "v_cvt_pk_bf16_f32 v137, v138, v139\n"
    "v_cvt_pk_bf16_f32 v138, v140, v141\n"
    "v_cvt_pk_bf16_f32 v139, v142, v143\n"
    "s_nop 1\n"
    "v_mfma_f32_16x16x32_bf16 v[32:35], a[96:99], v[136:139], v[32:35]\n"
    "v_mfma_f32_16x16x32_bf16 v[36:39], a[100:103], v[136:139], v[36:39]\n"
    "v_mfma_f32_16x16x32_bf16 v[40:43], a[104:107], v[136:139], v[40:43]\n"
    "v_mfma_f32_16x16x32_bf16 v[44:47], a[108:111], v[136:139], v[44:47]\n"
    "v_mfma_f32_16x16x32_bf16 v[48:51], a[112:115], v[136:139], v[48:51]\n"
    "v_mfma_f32_16x16x32_bf16 v[52:55], a[116:119], v[136:139], v[52:55]\n"
    "v_mfma_f32_16x16x32_bf16 v[56:59], a[120:123], v[136:139], v[56:59]\n"
    "v_mfma_f32_16x16x32_bf16 v[60:63], a[124:127], v[136:139], v[60:63]\n"
    "v_mfma_f32_16x16x32_bf16 v[212:215], a[128:131], v[136:139], v[212:215]\n"
    "s_add_u32 m0, s55, 81920\n"
    "s_nop 0\n"
    "buffer_load_dwordx4 v240, s[44:47], s73 offen offset:0 nt lds\n"
    "buffer_load_dwordx4 v241, s[44:47], s73 offen offset:1024 nt lds\n"
    "buffer_load_dwordx4 v240, s[44:47], s73 offen offset:2048 nt lds\n"
    "buffer_load_dwordx4 v241, s[44:47], s73 offen offset:3072 nt lds\n"
    "s_waitcnt vmcnt(12)\n"
    "ds_read_b128 v[96:99], v176 offset:8192\n"
    "ds_read_b128 v[100:103], v177 offset:8192\n"
    "ds_read_b128 v[104:107], v178 offset:8192\n"
    "ds_read_b128 v[108:111], v179 offset:8192\n"
    "s_waitcnt lgkmcnt(3)\n"
    "v_lshlrev_b32 v112, 16, v96\n"
    "v_and_b32 v113, s69, v96\n"
    "v_lshlrev_b32 v114, 16, v97\n"
    "v_and_b32 v115, s69, v97\n"
    "v_lshlrev_b32 v116, 16, v98\n"
    "v_and_b32 v117, s69, v98\n"
    "v_lshlrev_b32 v118, 16, v99\n"
    "v_and_b32 v119, s69, v99\n"
    "v_pk_mul_f32 v[112:113], v[112:113], s[62:63]\n"
    "v_pk_mul_f32 v[114:115], v[114:115], s[62:63]\n"
    "v_pk_mul_f32 v[116:117], v[116:117], s[62:63]\n"
    "v_pk_mul_f32 v[118:119], v[118:119], s[62:63]\n"
    "v_cvt_pk_bf16_f32 v96, v112, v113\n"
    "v_cvt_pk_bf16_f32 v97, v114, v115\n"
    "v_cvt_pk_bf16_f32 v98, v116, v117\n"
    "v_cvt_pk_bf16_f32 v99, v118, v119\n"
    "v_accvgpr_write_b32 a32, v96\n"
    "v_accvgpr_write_b32 a33, v97\n"
    "v_accvgpr_write_b32 a34, v98\n"
    "v_accvgpr_write_b32 a35, v99\n"
    "s_waitcnt lgkmcnt(2)\n"
    "v_lshlrev_b32 v120, 16, v100\n"
    "v_and_b32 v121, s69, v100\n"
    "v_lshlrev_b32 v122, 16, v101\n"
    "v_and_b32 v123, s69, v101\n"
    "v_lshlrev_b32 v124, 16, v102\n"
    "v_and_b32 v125, s69, v102\n"
    "v_lshlrev_b32 v126, 16, v103\n"
    "v_and_b32 v127, s69, v103\n"
    "v_pk_mul_f32 v[120:121], v[120:121], s[62:63]\n"
    "v_pk_mul_f32 v[122:123], v[122:123], s[62:63]\n"
    "v_pk_mul_f32 v[124:125], v[124:125], s[62:63]\n"
    "v_pk_mul_f32 v[126:127], v[126:127], s[62:63]\n"
    "v_cvt_pk_bf16_f32 v100, v120, v121\n"
    "v_cvt_pk_bf16_f32 v101, v122, v123\n"
    "v_cvt_pk_bf16_f32 v102, v124, v125\n"
    "v_cvt_pk_bf16_f32 v103, v126, v127\n"
    "v_accvgpr_write_b32 a36, v100\n"
    "v_accvgpr_write_b32 a37, v101\n"
    "v_accvgpr_write_b32 a38, v102\n"
    "v_accvgpr_write_b32 a39, v103\n"
    "s_waitcnt lgkmcnt(1)\n"
    "v_lshlrev_b32 v112, 16, v104\n"
    "v_and_b32 v113, s69, v104\n"
    "v_lshlrev_b32 v114, 16, v105\n"
    "v_and_b32 v115, s69, v105\n"
    "v_lshlrev_b32 v116, 16, v106\n"
    "v_and_b32 v117, s69, v106\n"
    "v_lshlrev_b32 v118, 16, v107\n"
    "v_and_b32 v119, s69, v107\n"
    "v_pk_mul_f32 v[112:113], v[112:113], s[62:63]\n"
    "v_pk_mul_f32 v[114:115], v[114:115], s[62:63]\n"
    "v_pk_mul_f32 v[116:117], v[116:117], s[62:63]\n"
    "v_pk_mul_f32 v[118:119], v[118:119], s[62:63]\n"
    "v_cvt_pk_bf16_f32 v104, v112, v113\n"
    "v_cvt_pk_bf16_f32 v105, v114, v115\n"
    "v_cvt_pk_bf16_f32 v106, v116, v117\n"
    "v_cvt_pk_bf16_f32 v107, v118, v119\n"
    "v_accvgpr_write_b32 a40, v104\n"
    "v_accvgpr_write_b32 a41, v105\n"
    "v_accvgpr_write_b32 a42, v106\n"
    "v_accvgpr_write_b32 a43, v107\n"
    "s_waitcnt lgkmcnt(0)\n"
    "v_lshlrev_b32 v120, 16, v108\n"
    "v_and_b32 v121, s69, v108\n"
    "v_lshlrev_b32 v122, 16, v109\n"
    "v_and_b32 v123, s69, v109\n"
    "v_lshlrev_b32 v124, 16, v110\n"
    "v_and_b32 v125, s69, v110\n"
    "v_lshlrev_b32 v126, 16, v111\n"
    "v_and_b32 v127, s69, v111\n"
    "v_pk_mul_f32 v[120:121], v[120:121], s[62:63]\n"
    "v_pk_mul_f32 v[122:123], v[122:123], s[62:63]\n"
    "v_pk_mul_f32 v[124:125], v[124:125], s[62:63]\n"
    "v_pk_mul_f32 v[126:127], v[126:127], s[62:63]\n"
    "v_cvt_pk_bf16_f32 v108, v120, v121\n"
    "v_cvt_pk_bf16_f32 v109, v122, v123\n"
    "v_cvt_pk_bf16_f32 v110, v124, v125\n"
    "v_cvt_pk_bf16_f32 v111, v126, v127\n"
    "v_accvgpr_write_b32 a44, v108\n"
    "v_accvgpr_write_b32 a45, v109\n"
    "v_accvgpr_write_b32 a46, v110\n"
    "v_accvgpr_write_b32 a47, v111\n"
    "v_mfma_f32_16x16x32_bf16 v[160:163], a[64:67], a[32:35], 0\n"
    "v_mfma_f32_16x16x32_bf16 v[164:167], a[80:83], a[32:35], 0\n"
    "v_mfma_f32_16x16x32_bf16 v[144:147], a[132:135], a[32:35], 0\n"
    "v_mfma_f32_16x16x32_bf16 v[148:151], a[148:151], a[32:35], 0\n"
    "v_mfma_f32_16x16x32_bf16 v[160:163], a[68:71], a[36:39], v[160:163]\n"
    "v_mfma_f32_16x16x32_bf16 v[164:167], a[84:87], a[36:39], v[164:167]\n"
    "v_mfma_f32_16x16x32_bf16 v[144:147], a[136:139], a[36:39], v[144:147]\n"
    "v_mfma_f32_16x16x32_bf16 v[148:151], a[152:155], a[36:39], v[148:151]\n"
    "v_mfma_f32_16x16x32_bf16 v[160:163], a[72:75], a[40:43], v[160:163]\n"
    "v_mfma_f32_16x16x32_bf16 v[164:167], a[88:91], a[40:43], v[164:167]\n"
    "v_mfma_f32_16x16x32_bf16 v[144:147], a[140:143], a[40:43], v[144:147]\n"
    "v_mfma_f32_16x16x32_bf16 v[148:151], a[156:159], a[40:43], v[148:151]\n"
    "v_mfma_f32_16x16x32_bf16 v[160:163], a[76:79], a[44:47], v[160:163]\n"
    "v_mfma_f32_16x16x32_bf16 v[164:167], a[92:95], a[44:47], v[164:167]\n"
    "v_mfma_f32_16x16x32_bf16 v[144:147], a[144:147], a[44:47], v[144:147]\n"
    "v_mfma_f32_16x16x32_bf16 v[148:151], a[160:163], a[44:47], v[148:151]\n"
    "s_nop 4\n"
    "v_max3_f32 v243, v160, v161, v162\n"
    "v_max3_f32 v244, v163, v164, v165\n"
    "v_max3_f32 v243, v243, v166, v167\n"
    "v_max3_f32 v244, v244, v144, v145\n"
    "v_max3_f32 v243, v243, v146, v147\n"
    "v_max3_f32 v244, v244, v148, v149\n"
    "v_max3_f32 v243, v243, v150, v151\n"
    "v_max_f32 v243, v243, v244\n"
    "v_mov_b32 v244, v243\n"
    "s_nop 1\n"
    "v_permlane16_swap_b32 v244, v243\n"
    "v_max_f32 v243, v244, v243\n"
    "v_mov_b32 v244, v243\n"
    "s_nop 1\n"
    "v_permlane32_swap_b32 v244, v243\n"
    "v_max_f32 v243, v244, v243\n"
    "v_add_f32 v243, 0xc1e00000, v243\n"
    "v_sub_f32 v160, v160, v243\n"
    "v_sub_f32 v161, v161, v243\n"
    "v_sub_f32 v162, v162, v243\n"
    "v_sub_f32 v163, v163, v243\n"
    "v_sub_f32 v164, v164, v243\n"
    "v_sub_f32 v165, v165, v243\n"
    "v_sub_f32 v166, v166, v243\n"
    "v_sub_f32 v167, v167, v243\n"
    "v_exp_f32 v160, v160\n"
    "v_sub_f32 v144, v144, v243\n"
    "v_exp_f32 v161, v161\n"
    "v_sub_f32 v145, v145, v243\n"
    "v_exp_f32 v162, v162\n"
    "v_sub_f32 v146, v146, v243\n"
    "v_exp_f32 v163, v163\n"
    "v_sub_f32 v147, v147, v243\n"
    "v_exp_f32 v164, v164\n"
    "v_sub_f32 v148, v148, v243\n"
    "v_exp_f32 v165, v165\n"
    "v_sub_f32 v149, v149, v243\n"
    "v_exp_f32 v166, v166\n"
    "v_sub_f32 v150, v150, v243\n"
    "v_exp_f32 v167, v167\n"
    "v_sub_f32 v151, v151, v243\n"
    "v_cvt_pk_bf16_f32 v160, v160, v161\n"
    "v_sub_f32 v200, 0, v243\n"
    "v_cvt_pk_bf16_f32 v161, v162, v163\n"
    "v_sub_f32 v201, 0, v243\n"
    "v_cvt_pk_bf16_f32 v162, v164, v165\n"
    "v_sub_f32 v202, 0, v243\n"
    "v_cvt_pk_bf16_f32 v163, v166, v167\n"
    "v_sub_f32 v203, 0, v243\n"
    "s_nop 0\n"
    "v_mfma_f32_16x16x32_bf16 v[64:67], a[164:167], v[160:163], 0\n"
    "v_exp_f32 v144, v144\n"
    "v_mfma_f32_16x16x32_bf16 v[68:71], a[168:171], v[160:163], 0\n"
    "v_exp_f32 v145, v145\n"
    "v_mfma_f32_16x16x32_bf16 v[72:75], a[172:175], v[160:163], 0\n"
    "v_exp_f32 v146, v146\n"
    "v_mfma_f32_16x16x32_bf16 v[76:79], a[176:179], v[160:163], 0\n"
    "v_exp_f32 v147, v147\n"
    "v_mfma_f32_16x16x32_bf16 v[80:83], a[180:183], v[160:163], 0\n"
    "v_exp_f32 v148, v148\n"
    "v_mfma_f32_16x16x32_bf16 v[84:87], a[184:187], v[160:163], 0\n"
    "v_exp_f32 v149, v149\n"
    "v_mfma_f32_16x16x32_bf16 v[88:91], a[188:191], v[160:163], 0\n"
    "v_exp_f32 v150, v150\n"
    "v_mfma_f32_16x16x32_bf16 v[92:95], a[192:195], v[160:163], 0\n"
    "v_exp_f32 v151, v151\n"
    "v_mfma_f32_16x16x32_bf16 v[216:219], a[128:131], v[160:163], 0\n"
    "v_cvt_pk_bf16_f32 v144, v144, v145\n"
    "v_cvt_pk_bf16_f32 v145, v146, v147\n"
    "v_cvt_pk_bf16_f32 v146, v148, v149\n"
    "v_cvt_pk_bf16_f32 v147, v150, v151\n"
    "s_add_u32 m0, s55, 32768\n"
    "s_nop 0\n"
    "buffer_load_dwordx4 v236, s[40:43], s74 offen offset:0 nt lds\n"
    "buffer_load_dwordx4 v237, s[40:43], s74 offen offset:1024 nt lds\n"
    "buffer_load_dwordx4 v238, s[40:43], s74 offen offset:2048 nt lds\n"
    "buffer_load_dwordx4 v239, s[40:43], s74 offen offset:3072 nt lds\n"
    "s_waitcnt vmcnt(12)\n"
    "ds_read_b128 v[96:99], v176 offset:12288\n"
    "ds_read_b128 v[100:103], v177 offset:12288\n"
    "ds_read_b128 v[104:107], v178 offset:12288\n"
    "ds_read_b128 v[108:111], v179 offset:12288\n"
    "s_waitcnt lgkmcnt(3)\n"
    "v_lshlrev_b32 v112, 16, v96\n"
    "v_and_b32 v113, s69, v96\n"
    "v_lshlrev_b32 v114, 16, v97\n"
    "v_and_b32 v115, s69, v97\n"
    "v_lshlrev_b32 v116, 16, v98\n"
    "v_and_b32 v117, s69, v98\n"
    "v_lshlrev_b32 v118, 16, v99\n"
    "v_and_b32 v119, s69, v99\n"
    "v_pk_mul_f32 v[112:113], v[112:113], s[62:63]\n"
    "v_pk_mul_f32 v[114:115], v[114:115], s[62:63]\n"
    "v_pk_mul_f32 v[116:117], v[116:117], s[62:63]\n"
    "v_pk_mul_f32 v[118:119], v[118:119], s[62:63]\n"
    "v_cvt_pk_bf16_f32 v96, v112, v113\n"
    "v_cvt_pk_bf16_f32 v97, v114, v115\n"
    "v_cvt_pk_bf16_f32 v98, v116, v117\n"
    "v_cvt_pk_bf16_f32 v99, v118, v119\n"
    "v_accvgpr_write_b32 a48, v96\n"
    "v_accvgpr_write_b32 a49, v97\n"
    "v_accvgpr_write_b32 a50, v98\n"
    "v_accvgpr_write_b32 a51, v99\n"
    "s_waitcnt lgkmcnt(2)\n"
    "v_lshlrev_b32 v120, 16, v100\n"
    "v_and_b32 v121, s69, v100\n"
    "v_lshlrev_b32 v122, 16, v101\n"
    "v_and_b32 v123, s69, v101\n"
    "v_lshlrev_b32 v124, 16, v102\n"
    "v_and_b32 v125, s69, v102\n"
    "v_lshlrev_b32 v126, 16, v103\n"
    "v_and_b32 v127, s69, v103\n"
    "v_pk_mul_f32 v[120:121], v[120:121], s[62:63]\n"
    "v_pk_mul_f32 v[122:123], v[122:123], s[62:63]\n"
    "v_pk_mul_f32 v[124:125], v[124:125], s[62:63]\n"
    "v_pk_mul_f32 v[126:127], v[126:127], s[62:63]\n"
    "v_cvt_pk_bf16_f32 v100, v120, v121\n"
    "v_cvt_pk_bf16_f32 v101, v122, v123\n"
    "v_cvt_pk_bf16_f32 v102, v124, v125\n"
    "v_cvt_pk_bf16_f32 v103, v126, v127\n"
    "v_accvgpr_write_b32 a52, v100\n"
    "v_accvgpr_write_b32 a53, v101\n"
    "v_accvgpr_write_b32 a54, v102\n"
    "v_accvgpr_write_b32 a55, v103\n"
    "s_waitcnt lgkmcnt(1)\n"
    "v_lshlrev_b32 v112, 16, v104\n"
    "v_and_b32 v113, s69, v104\n"
    "v_lshlrev_b32 v114, 16, v105\n"
    "v_and_b32 v115, s69, v105\n"
    "v_lshlrev_b32 v116, 16, v106\n"
    "v_and_b32 v117, s69, v106\n"
    "v_lshlrev_b32 v118, 16, v107\n"
    "v_and_b32 v119, s69, v107\n"
    "v_pk_mul_f32 v[112:113], v[112:113], s[62:63]\n"
    "v_pk_mul_f32 v[114:115], v[114:115], s[62:63]\n"
    "v_pk_mul_f32 v[116:117], v[116:117], s[62:63]\n"
    "v_pk_mul_f32 v[118:119], v[118:119], s[62:63]\n"
    "v_cvt_pk_bf16_f32 v104, v112, v113\n"
    "v_cvt_pk_bf16_f32 v105, v114, v115\n"
    "v_cvt_pk_bf16_f32 v106, v116, v117\n"
    "v_cvt_pk_bf16_f32 v107, v118, v119\n"
    "v_accvgpr_write_b32 a56, v104\n"
    "v_accvgpr_write_b32 a57, v105\n"
    "v_accvgpr_write_b32 a58, v106\n"
    "v_accvgpr_write_b32 a59, v107\n"
    "s_waitcnt lgkmcnt(0)\n"
    "v_lshlrev_b32 v120, 16, v108\n"
    "v_and_b32 v121, s69, v108\n"
    "v_lshlrev_b32 v122, 16, v109\n"
    "v_and_b32 v123, s69, v109\n"
    "v_lshlrev_b32 v124, 16, v110\n"
    "v_and_b32 v125, s69, v110\n"
    "v_lshlrev_b32 v126, 16, v111\n"
    "v_and_b32 v127, s69, v111\n"
    "v_pk_mul_f32 v[120:121], v[120:121], s[62:63]\n"
    "v_pk_mul_f32 v[122:123], v[122:123], s[62:63]\n"
    "v_pk_mul_f32 v[124:125], v[124:125], s[62:63]\n"
    "v_pk_mul_f32 v[126:127], v[126:127], s[62:63]\n"
    "v_cvt_pk_bf16_f32 v108, v120, v121\n"
    "v_cvt_pk_bf16_f32 v109, v122, v123\n"
    "v_cvt_pk_bf16_f32 v110, v124, v125\n"
    "v_cvt_pk_bf16_f32 v111, v126, v127\n"
    "v_accvgpr_write_b32 a60, v108\n"
    "v_accvgpr_write_b32 a61, v109\n"
    "v_accvgpr_write_b32 a62, v110\n"
    "v_accvgpr_write_b32 a63, v111\n"
    "v_mfma_f32_16x16x32_bf16 v[160:163], a[64:67], a[48:51], 0\n"
    "v_mfma_f32_16x16x32_bf16 v[164:167], a[80:83], a[48:51], 0\n"
    "v_mfma_f32_16x16x32_bf16 v[152:155], a[132:135], a[48:51], 0\n"
    "v_mfma_f32_16x16x32_bf16 v[156:159], a[148:151], a[48:51], 0\n"
    "v_mfma_f32_16x16x32_bf16 v[160:163], a[68:71], a[52:55], v[160:163]\n"
    "v_mfma_f32_16x16x32_bf16 v[164:167], a[84:87], a[52:55], v[164:167]\n"
    "v_mfma_f32_16x16x32_bf16 v[152:155], a[136:139], a[52:55], v[152:155]\n"
    "v_mfma_f32_16x16x32_bf16 v[156:159], a[152:155], a[52:55], v[156:159]\n"
    "v_mfma_f32_16x16x32_bf16 v[160:163], a[72:75], a[56:59], v[160:163]\n"
    "v_mfma_f32_16x16x32_bf16 v[164:167], a[88:91], a[56:59], v[164:167]\n"
    "v_mfma_f32_16x16x32_bf16 v[152:155], a[140:143], a[56:59], v[152:155]\n"
    "v_mfma_f32_16x16x32_bf16 v[156:159], a[156:159], a[56:59], v[156:159]\n"
    "v_mfma_f32_16x16x32_bf16 v[160:163], a[76:79], a[60:63], v[160:163]\n"
    "v_mfma_f32_16x16x32_bf16 v[164:167], a[92:95], a[60:63], v[164:167]\n"
    "v_mfma_f32_16x16x32_bf16 v[152:155], a[144:147], a[60:63], v[152:155]\n"
    "v_mfma_f32_16x16x32_bf16 v[156:159], a[160:163], a[60:63], v[156:159]\n"
    "s_nop 4\n"
    "v_max3_f32 v243, v160, v161, v162\n"
    "v_max3_f32 v244, v163, v164, v165\n"
    "v_max3_f32 v243, v243, v166, v167\n"
    "v_max3_f32 v244, v244, v152, v153\n"
    "v_max3_f32 v243, v243, v154, v155\n"
    "v_max3_f32 v244, v244, v156, v157\n"
    "v_max3_f32 v243, v243, v158, v159\n"
    "v_max_f32 v243, v243, v244\n"
    "v_mov_b32 v244, v243\n"
    "s_nop 1\n"
    "v_permlane16_swap_b32 v244, v243\n"
    "v_max_f32 v243, v244, v243\n"
    "v_mov_b32 v244, v243\n"
    "s_nop 1\n"
    "v_permlane32_swap_b32 v244, v243\n"
    "v_max_f32 v243, v244, v243\n"
    "v_add_f32 v243, 0xc1e00000, v243\n"
    "v_sub_f32 v160, v160, v243\n"
    "v_sub_f32 v161, v161, v243\n"
    "v_sub_f32 v162, v162, v243\n"
    "v_sub_f32 v163, v163, v243\n"
    "v_sub_f32 v164, v164, v243\n"
    "v_sub_f32 v165, v165, v243\n"
    "v_sub_f32 v166, v166, v243\n"
    "v_sub_f32 v167, v167, v243\n"
    "v_exp_f32 v160, v160\n"
    "v_sub_f32 v152, v152, v243\n"
    "v_exp_f32 v161, v161\n"
    "v_sub_f32 v153, v153, v243\n"
    "v_exp_f32 v162, v162\n"
    "v_sub_f32 v154, v154, v243\n"
    "v_exp_f32 v163, v163\n"
    "v_sub_f32 v155, v155, v243\n"
    "v_exp_f32 v164, v164\n"
    "v_sub_f32 v156, v156, v243\n"
    "v_exp_f32 v165, v165\n"
    "v_sub_f32 v157, v157, v243\n"
    "v_exp_f32 v166, v166\n"
    "v_sub_f32 v158, v158, v243\n"
    "v_exp_f32 v167, v167\n"
    "v_sub_f32 v159, v159, v243\n"
    "v_cvt_pk_bf16_f32 v160, v160, v161\n"
    "v_sub_f32 v204, 0, v243\n"
    "v_cvt_pk_bf16_f32 v161, v162, v163\n"
    "v_sub_f32 v205, 0, v243\n"
    "v_cvt_pk_bf16_f32 v162, v164, v165\n"
    "v_sub_f32 v206, 0, v243\n"
    "v_cvt_pk_bf16_f32 v163, v166, v167\n"
    "v_sub_f32 v207, 0, v243\n"
    "s_waitcnt vmcnt(8)\n"
    "s_barrier\n"
    "ds_read_b128 a[64:67], v224 offset:16384\n"
    "ds_read_b128 a[68:71], v225 offset:16384\n"
    "ds_read_b128 a[72:75], v226 offset:16384\n"
    "ds_read_b128 a[76:79], v227 offset:16384\n"
    "ds_read_b128 a[80:83], v224 offset:20480\n"
    "ds_read_b128 a[84:87], v225 offset:20480\n"
    "ds_read_b128 a[88:91], v226 offset:20480\n"
    "ds_read_b128 a[92:95], v227 offset:20480\n"
    "s_add_u32 m0, s55, 98304\n"
    "s_nop 0\n"
    "buffer_load_dwordx4 v240, s[44:47], s74 offen offset:0 nt lds\n"
    "buffer_load_dwordx4 v241, s[44:47], s74 offen offset:1024 nt lds\n"
    "buffer_load_dwordx4 v240, s[44:47], s74 offen offset:2048 nt lds\n"
    "buffer_load_dwordx4 v241, s[44:47], s74 offen offset:3072 nt lds\n"
    "v_mfma_f32_16x16x32_bf16 v[96:99], a[164:167], v[160:163], 0\n"
    "v_exp_f32 v152, v152\n"
    "v_mfma_f32_16x16x32_bf16 v[100:103], a[168:171], v[160:163], 0\n"
    "v_exp_f32 v153, v153\n"
    "v_mfma_f32_16x16x32_bf16 v[104:107], a[172:175], v[160:163], 0\n"
    "v_exp_f32 v154, v154\n"
    "v_mfma_f32_16x16x32_bf16 v[108:111], a[176:179], v[160:163], 0\n"
    "v_exp_f32 v155, v155\n"
    "v_mfma_f32_16x16x32_bf16 v[112:115], a[180:183], v[160:163], 0\n"
    "v_exp_f32 v156, v156\n"
    "v_mfma_f32_16x16x32_bf16 v[116:119], a[184:187], v[160:163], 0\n"
    "v_exp_f32 v157, v157\n"
    "v_mfma_f32_16x16x32_bf16 v[120:123], a[188:191], v[160:163], 0\n"
    "v_exp_f32 v158, v158\n"
    "v_mfma_f32_16x16x32_bf16 v[124:127], a[192:195], v[160:163], 0\n"
    "v_exp_f32 v159, v159\n"
    "v_mfma_f32_16x16x32_bf16 v[220:223], a[128:131], v[160:163], 0\n"
    "v_cvt_pk_bf16_f32 v152, v152, v153\n"
    "v_cvt_pk_bf16_f32 v153, v154, v155\n"
    "v_cvt_pk_bf16_f32 v154, v156, v157\n"
    "v_cvt_pk_bf16_f32 v155, v158, v159\n"
    "s_waitcnt lgkmcnt(0)\n"
    "v_mfma_f32_16x16x32_bf16 v[128:131], a[64:67], a[0:3], v[192:195]\n"
    "s_nop 3\n"
    "v_mfma_f32_16x16x32_bf16 v[136:139], a[64:67], a[16:19], v[196:199]\n"
    "v_mfma_f32_16x16x32_bf16 v[128:131], a[68:71], a[4:7], v[128:131]\n"
    "v_mfma_f32_16x16x32_bf16 v[136:139], a[68:71], a[20:23], v[136:139]\n"
    "v_mfma_f32_16x16x32_bf16 v[128:131], a[72:75], a[8:11], v[128:131]\n"
    "s_nop 3\n"
    "v_mfma_f32_16x16x32_bf16 v[136:139], a[72:75], a[24:27], v[136:139]\n"
    "v_mfma_f32_16x16x32_bf16 v[128:131], a[76:79], a[12:15], v[128:131]\n"
    "v_mfma_f32_16x16x32_bf16 v[136:139], a[76:79], a[28:31], v[136:139]\n"
    "v_mfma_f32_16x16x32_bf16 v[132:135], a[80:83], a[0:3], v[192:195]\n"
    "s_nop 3\n"
    "v_mfma_f32_16x16x32_bf16 v[140:143], a[80:83], a[16:19], v[196:199]\n"
    "v_mfma_f32_16x16x32_bf16 v[132:135], a[84:87], a[4:7], v[132:135]\n"
    "s_nop 3\n"
    "v_mfma_f32_16x16x32_bf16 v[140:143], a[84:87], a[20:23], v[140:143]\n"
    "v_mfma_f32_16x16x32_bf16 v[132:135], a[88:91], a[8:11], v[132:135]\n"
    "v_exp_f32 v128, v128\n"
    "v_mfma_f32_16x16x32_bf16 v[140:143], a[88:91], a[24:27], v[140:143]\n"
    "buffer_load_dword v248, off, s[48:51], s58\n"
    "v_mfma_f32_16x16x32_bf16 v[132:135], a[92:95], a[12:15], v[132:135]\n"
    "v_exp_f32 v129, v129\n"
    "s_nop 3\n"
    "v_mfma_f32_16x16x32_bf16 v[140:143], a[92:95], a[28:31], v[140:143]\n"
    "s_add_u32 m0, s55, 49152\n"
    "v_cvt_pk_bf16_f32 v128, v128, v129\n"
    "v_mfma_f32_16x16x32_bf16 v[64:67], a[96:99], v[144:147], v[64:67]\n"
    "v_exp_f32 v130, v130\n"
    "v_mfma_f32_16x16x32_bf16 v[96:99], a[96:99], v[152:155], v[96:99]\n"
    "v_mfma_f32_16x16x32_bf16 v[68:71], a[100:103], v[144:147], v[68:71]\n"
    "v_exp_f32 v131, v131\n"
    "s_nop 3\n"
    "v_mfma_f32_16x16x32_bf16 v[100:103], a[100:103], v[152:155], v[100:103]\n"
    "v_cvt_pk_bf16_f32 v129, v130, v131\n"
    "v_mfma_f32_16x16x32_bf16 v[72:75], a[104:107], v[144:147], v[72:75]\n"
    "v_exp_f32 v136, v136\n"
    "v_mfma_f32_16x16x32_bf16 v[104:107], a[104:107], v[152:155], v[104:107]\n"
    "v_mfma_f32_16x16x32_bf16 v[76:79], a[108:111], v[144:147], v[76:79]\n"
    "v_exp_f32 v137, v137\n"
    "s_nop 3\n"
    "v_mfma_f32_16x16x32_bf16 v[108:111], a[108:111], v[152:155], v[108:111]\n"
    "v_cvt_pk_bf16_f32 v136, v136, v137\n"
    "v_mfma_f32_16x16x32_bf16 v[80:83], a[112:115], v[144:147], v[80:83]\n"
    "v_exp_f32 v138, v138\n"
    "s_nop 3\n"
    "v_mfma_f32_16x16x32_bf16 v[112:115], a[112:115], v[152:155], v[112:115]\n"
    "v_mfma_f32_16x16x32_bf16 v[84:87], a[116:119], v[144:147], v[84:87]\n"
    "v_exp_f32 v139, v139\n"
    "v_mfma_f32_16x16x32_bf16 v[116:119], a[116:119], v[152:155], v[116:119]\n"
    "v_cvt_pk_bf16_f32 v137, v138, v139\n"
    "v_mfma_f32_16x16x32_bf16 v[88:91], a[120:123], v[144:147], v[88:91]\n"
    "v_exp_f32 v132, v132\n"
    "s_nop 3\n"
    "v_mfma_f32_16x16x32_bf16 v[120:123], a[120:123], v[152:155], v[120:123]\n"
    "v_mfma_f32_16x16x32_bf16 v[92:95], a[124:127], v[144:147], v[92:95]\n"
    "v_exp_f32 v133, v133\n"
    "v_mfma_f32_16x16x32_bf16 v[124:127], a[124:127], v[152:155], v[124:127]\n"
    "v_cvt_pk_bf16_f32 v130, v132, v133\n"
    "v_mfma_f32_16x16x32_bf16 v[216:219], a[128:131], v[144:147], v[216:219]\n"
    "v_exp_f32 v134, v134\n"
    "s_nop 3\n"
    "v_mfma_f32_16x16x32_bf16 v[220:223], a[128:131], v[152:155], v[220:223]\n"
    "v_mfma_f32_16x16x32_bf16 v[144:147], a[64:67], a[32:35], v[200:203]\n"
    "v_exp_f32 v135, v135\n"
    "s_nop 3\n"
    "v_mfma_f32_16x16x32_bf16 v[152:155], a[64:67], a[48:51], v[204:207]\n"
    "v_cvt_pk_bf16_f32 v131, v134, v135\n"
    "v_mfma_f32_16x16x32_bf16 v[144:147], a[68:71], a[36:39], v[144:147]\n"
    "v_exp_f32 v140, v140\n"
    "v_mfma_f32_16x16x32_bf16 v[152:155], a[68:71], a[52:55], v[152:155]\n"
    "v_mfma_f32_16x16x32_bf16 v[144:147], a[72:75], a[40:43], v[144:147]\n"
    "v_exp_f32 v141, v141\n"
    "s_nop 3\n"
    "v_mfma_f32_16x16x32_bf16 v[152:155], a[72:75], a[56:59], v[152:155]\n"
    "v_cvt_pk_bf16_f32 v138, v140, v141\n"
    "v_mfma_f32_16x16x32_bf16 v[144:147], a[76:79], a[44:47], v[144:147]\n"
    "v_exp_f32 v142, v142\n"
    "v_mfma_f32_16x16x32_bf16 v[152:155], a[76:79], a[60:63], v[152:155]\n"
    "buffer_load_dwordx4 v236, s[40:43], s75 offen offset:0 nt lds\n"
    "v_mfma_f32_16x16x32_bf16 v[148:151], a[80:83], a[32:35], v[200:203]\n"
    "v_exp_f32 v143, v143\n"
    "s_nop 3\n"
    "v_mfma_f32_16x16x32_bf16 v[156:159], a[80:83], a[48:51], v[204:207]\n"
    "s_waitcnt vmcnt(10)\n"
    "s_barrier\n"
    "ds_read_b64_tr_b16 a[96:97], v228 offset:16384\n"
    "ds_read_b64_tr_b16 a[98:99], v228 offset:20480\n"
    "ds_read_b64_tr_b16 a[100:101], v229 offset:16384\n"
    "ds_read_b64_tr_b16 a[102:103], v229 offset:20480\n"
    "ds_read_b64_tr_b16 a[104:105], v230 offset:16384\n"
    "ds_read_b64_tr_b16 a[106:107], v230 offset:20480\n"
    "ds_read_b64_tr_b16 a[108:109], v231 offset:16384\n"
    "ds_read_b64_tr_b16 a[110:111], v231 offset:20480\n"
    "ds_read_b64_tr_b16 a[112:113], v232 offset:16384\n"
    "ds_read_b64_tr_b16 a[114:115], v232 offset:20480\n"
    "ds_read_b64_tr_b16 a[116:117], v233 offset:16384\n"
    "ds_read_b64_tr_b16 a[118:119], v233 offset:20480\n"
    "ds_read_b64_tr_b16 a[120:121], v234 offset:16384\n"
    "ds_read_b64_tr_b16 a[122:123], v234 offset:20480\n"
    "ds_read_b64_tr_b16 a[124:125], v235 offset:16384\n"
    "ds_read_b64_tr_b16 a[126:127], v235 offset:20480\n"
    "v_cvt_pk_bf16_f32 v139, v142, v143\n"
    "v_mfma_f32_16x16x32_bf16 v[148:151], a[84:87], a[36:39], v[148:151]\n"
    "buffer_load_dwordx4 v237, s[40:43], s75 offen offset:1024 nt lds\n"
    "s_nop 3\n"
    "v_mfma_f32_16x16x32_bf16 v[156:159], a[84:87], a[52:55], v[156:159]\n"
    "v_mfma_f32_16x16x32_bf16 v[148:151], a[88:91], a[40:43], v[148:151]\n"
    "v_exp_f32 v144, v144\n"
    "v_mfma_f32_16x16x32_bf16 v[156:159], a[88:91], a[56:59], v[156:159]\n"
    "ds_read_b128 a[64:67], v224 offset:24576\n"
    "ds_read_b128 a[68:71], v225 offset:24576\n"
    "v_mfma_f32_16x16x32_bf16 v[148:151], a[92:95], a[44:47], v[148:151]\n"
    "v_exp_f32 v145, v145\n"
    "s_nop 3\n"
    "v_mfma_f32_16x16x32_bf16 v[156:159], a[92:95], a[60:63], v[156:159]\n"
    "s_waitcnt lgkmcnt(2)\n"
    "v_cvt_pk_bf16_f32 v144, v144, v145\n"
    "v_mfma_f32_16x16x32_bf16 v[0:3], a[96:99], v[128:131], v[0:3]\n"
    "v_exp_f32 v146, v146\n"
    "v_mfma_f32_16x16x32_bf16 v[32:35], a[96:99], v[136:139], v[32:35]\n"
    "ds_read_b128 a[72:75], v226 offset:24576\n"
    "ds_read_b128 a[76:79], v227 offset:24576\n"
    "v_mfma_f32_16x16x32_bf16 v[4:7], a[100:103], v[128:131], v[4:7]\n"
    "v_exp_f32 v147, v147\n"
    "s_nop 3\n"
    "v_mfma_f32_16x16x32_bf16 v[36:39], a[100:103], v[136:139], v[36:39]\n"
    "ds_read_b128 a[80:83], v224 offset:28672\n"
    "v_cvt_pk_bf16_f32 v145, v146, v147\n"
    "v_mfma_f32_16x16x32_bf16 v[8:11], a[104:107], v[128:131], v[8:11]\n"
    "v_exp_f32 v152, v152\n"
    "v_mfma_f32_16x16x32_bf16 v[40:43], a[104:107], v[136:139], v[40:43]\n"
    "ds_read_b128 a[84:87], v225 offset:28672\n"
    "ds_read_b128 a[88:91], v226 offset:28672\n"
    "v_mfma_f32_16x16x32_bf16 v[12:15], a[108:111], v[128:131], v[12:15]\n"
    "v_exp_f32 v153, v153\n"
    "s_nop 3\n"
    "v_mfma_f32_16x16x32_bf16 v[44:47], a[108:111], v[136:139], v[44:47]\n"
    "ds_read_b128 a[92:95], v227 offset:28672\n"
    "v_cvt_pk_bf16_f32 v152, v152, v153\n"
    "v_mfma_f32_16x16x32_bf16 v[16:19], a[112:115], v[128:131], v[16:19]\n"
    "v_exp_f32 v154, v154\n"
    "s_nop 3\n"
    "v_mfma_f32_16x16x32_bf16 v[48:51], a[112:115], v[136:139], v[48:51]\n"
    "buffer_load_dwordx4 v238, s[40:43], s75 offen offset:2048 nt lds\n"
    "v_mfma_f32_16x16x32_bf16 v[20:23], a[116:119], v[128:131], v[20:23]\n"
    "v_exp_f32 v155, v155\n"
    "v_mfma_f32_16x16x32_bf16 v[52:55], a[116:119], v[136:139], v[52:55]\n"
    "v_cvt_pk_bf16_f32 v153, v154, v155\n"
    "v_mfma_f32_16x16x32_bf16 v[24:27], a[120:123], v[128:131], v[24:27]\n"
    "v_exp_f32 v148, v148\n"
    "s_nop 3\n"
    "v_mfma_f32_16x16x32_bf16 v[56:59], a[120:123], v[136:139], v[56:59]\n"
    "buffer_load_dwordx4 v239, s[40:43], s75 offen offset:3072 nt lds\n"
    "v_mfma_f32_16x16x32_bf16 v[28:31], a[124:127], v[128:131], v[28:31]\n"
    "v_exp_f32 v149, v149\n"
    "v_mfma_f32_16x16x32_bf16 v[60:63], a[124:127], v[136:139], v[60:63]\n"
    "v_cvt_pk_bf16_f32 v146, v148, v149\n"
    "v_mfma_f32_16x16x32_bf16 v[208:211], a[128:131], v[128:131], v[208:211]\n"
    "v_exp_f32 v150, v150\n"
    "s_nop 3\n"
    "v_mfma_f32_16x16x32_bf16 v[212:215], a[128:131], v[136:139], v[212:215]\n"
    "s_waitcnt lgkmcnt(0)\n"
    "v_mfma_f32_16x16x32_bf16 v[128:131], a[64:67], a[0:3], v[192:195]\n"
    "v_exp_f32 v151, v151\n"
    "v_mfma_f32_16x16x32_bf16 v[136:139], a[64:67], a[16:19], v[196:199]\n"
    "v_cvt_pk_bf16_f32 v147, v150, v151\n"
    "v_mfma_f32_16x16x32_bf16 v[128:131], a[68:71], a[4:7], v[128:131]\n"
    "v_exp_f32 v156, v156\n"
    "s_nop 3\n"
    "v_mfma_f32_16x16x32_bf16 v[136:139], a[68:71], a[20:23], v[136:139]\n"
    "v_xor_b32 v224, 0x8000, v224\n"
    "v_xor_b32 v225, 0x8000, v225\n"
    "v_mfma_f32_16x16x32_bf16 v[128:131], a[72:75], a[8:11], v[128:131]\n"
    "v_exp_f32 v157, v157\n"
    "v_mfma_f32_16x16x32_bf16 v[136:139], a[72:75], a[24:27], v[136:139]\n"
    "v_cvt_pk_bf16_f32 v154, v156, v157\n"
    "v_mfma_f32_16x16x32_bf16 v[128:131], a[76:79], a[12:15], v[128:131]\n"
    "v_exp_f32 v158, v158\n"
    "s_nop 3\n"
    "v_mfma_f32_16x16x32_bf16 v[136:139], a[76:79], a[28:31], v[136:139]\n"
    "v_xor_b32 v226, 0x8000, v226\n"
    "v_xor_b32 v227, 0x8000, v227\n"
    "v_mfma_f32_16x16x32_bf16 v[132:135], a[80:83], a[0:3], v[192:195]\n"
    "v_exp_f32 v159, v159\n"
    "v_mfma_f32_16x16x32_bf16 v[140:143], a[80:83], a[16:19], v[196:199]\n"
    "v_cvt_pk_bf16_f32 v155, v158, v159\n"
    "v_mfma_f32_16x16x32_bf16 v[132:135], a[84:87], a[4:7], v[132:135]\n"
    "s_nop 3\n"
    "v_mfma_f32_16x16x32_bf16 v[140:143], a[84:87], a[20:23], v[140:143]\n"
    "v_mfma_f32_16x16x32_bf16 v[132:135], a[88:91], a[8:11], v[132:135]\n"
    "v_exp_f32 v128, v128\n"
    "v_mfma_f32_16x16x32_bf16 v[140:143], a[88:91], a[24:27], v[140:143]\n"
    "v_mfma_f32_16x16x32_bf16 v[132:135], a[92:95], a[12:15], v[132:135]\n"
    "v_exp_f32 v129, v129\n"
    "s_nop 3\n"
    "v_mfma_f32_16x16x32_bf16 v[140:143], a[92:95], a[28:31], v[140:143]\n"
    "s_add_u32 m0, s55, 114688\n"
    "v_cvt_pk_bf16_f32 v128, v128, v129\n"
    "v_mfma_f32_16x16x32_bf16 v[64:67], a[96:99], v[144:147], v[64:67]\n"
    "v_exp_f32 v130, v130\n"
    "v_mfma_f32_16x16x32_bf16 v[96:99], a[96:99], v[152:155], v[96:99]\n"
    "v_mfma_f32_16x16x32_bf16 v[68:71], a[100:103], v[144:147], v[68:71]\n"
    "v_exp_f32 v131, v131\n"
    "s_nop 3\n"
    "v_mfma_f32_16x16x32_bf16 v[100:103], a[100:103], v[152:155], v[100:103]\n"
    "ds_read_b64_tr_b16 a[96:97], v228 offset:24576\n"
    "v_cvt_pk_bf16_f32 v129, v130, v131\n"
    "v_mfma_f32_16x16x32_bf16 v[72:75], a[104:107], v[144:147], v[72:75]\n"
    "v_exp_f32 v136, v136\n"
    "v_mfma_f32_16x16x32_bf16 v[104:107], a[104:107], v[152:155], v[104:107]\n"
    "ds_read_b64_tr_b16 a[98:99], v228 offset:28672\n"
    "ds_read_b64_tr_b16 a[100:101], v229 offset:24576\n"
    "v_mfma_f32_16x16x32_bf16 v[76:79], a[108:111], v[144:147], v[76:79]\n"
    "v_exp_f32 v137, v137\n"
    "s_nop 3\n"
    "v_mfma_f32_16x16x32_bf16 v[108:111], a[108:111], v[152:155], v[108:111]\n"
    "ds_read_b64_tr_b16 a[102:103], v229 offset:28672\n"
    "v_cvt_pk_bf16_f32 v136, v136, v137\n"
    "v_mfma_f32_16x16x32_bf16 v[80:83], a[112:115], v[144:147], v[80:83]\n"
    "v_exp_f32 v138, v138\n"
    "v_mfma_f32_16x16x32_bf16 v[112:115], a[112:115], v[152:155], v[112:115]\n"
    "ds_read_b64_tr_b16 a[104:105], v230 offset:24576\n"
    "ds_read_b64_tr_b16 a[106:107], v230 offset:28672\n"
    "v_mfma_f32_16x16x32_bf16 v[84:87], a[116:119], v[144:147], v[84:87]\n"
    "v_exp_f32 v139, v139\n"
    "s_nop 3\n"
    "v_mfma_f32_16x16x32_bf16 v[116:119], a[116:119], v[152:155], v[116:119]\n"
    "ds_read_b64_tr_b16 a[108:109], v231 offset:24576\n"
    "v_cvt_pk_bf16_f32 v137, v138, v139\n"
    "v_mfma_f32_16x16x32_bf16 v[88:91], a[120:123], v[144:147], v[88:91]\n"
    "v_exp_f32 v132, v132\n"
    "v_mfma_f32_16x16x32_bf16 v[120:123], a[120:123], v[152:155], v[120:123]\n"
    "ds_read_b64_tr_b16 a[110:111], v231 offset:28672\n"
    "ds_read_b64_tr_b16 a[112:113], v232 offset:24576\n"
    "v_mfma_f32_16x16x32_bf16 v[92:95], a[124:127], v[144:147], v[92:95]\n"
    "v_exp_f32 v133, v133\n"
    "s_nop 3\n"
    "v_mfma_f32_16x16x32_bf16 v[124:127], a[124:127], v[152:155], v[124:127]\n"
    "ds_read_b64_tr_b16 a[114:115], v232 offset:28672\n"
    "v_cvt_pk_bf16_f32 v130, v132, v133\n"
    "v_mfma_f32_16x16x32_bf16 v[216:219], a[128:131], v[144:147], v[216:219]\n"
    "v_exp_f32 v134, v134\n"
    "v_mfma_f32_16x16x32_bf16 v[220:223], a[128:131], v[152:155], v[220:223]\n"
    "ds_read_b64_tr_b16 a[116:117], v233 offset:24576\n"
    "ds_read_b64_tr_b16 a[118:119], v233 offset:28672\n"
    "v_mfma_f32_16x16x32_bf16 v[144:147], a[64:67], a[32:35], v[200:203]\n"
    "v_exp_f32 v135, v135\n"
    "s_nop 3\n"
    "v_mfma_f32_16x16x32_bf16 v[152:155], a[64:67], a[48:51], v[204:207]\n"
    "ds_read_b64_tr_b16 a[120:121], v234 offset:24576\n"
    "v_cvt_pk_bf16_f32 v131, v134, v135\n"
    "v_mfma_f32_16x16x32_bf16 v[144:147], a[68:71], a[36:39], v[144:147]\n"
    "v_exp_f32 v140, v140\n"
    "v_mfma_f32_16x16x32_bf16 v[152:155], a[68:71], a[52:55], v[152:155]\n"
    "ds_read_b64_tr_b16 a[122:123], v234 offset:28672\n"
    "ds_read_b64_tr_b16 a[124:125], v235 offset:24576\n"
    "v_mfma_f32_16x16x32_bf16 v[144:147], a[72:75], a[40:43], v[144:147]\n"
    "v_exp_f32 v141, v141\n"
    "s_nop 3\n"
    "v_mfma_f32_16x16x32_bf16 v[152:155], a[72:75], a[56:59], v[152:155]\n"
    "ds_read_b64_tr_b16 a[126:127], v235 offset:28672\n"
    "v_cvt_pk_bf16_f32 v138, v140, v141\n"
    "v_mfma_f32_16x16x32_bf16 v[144:147], a[76:79], a[44:47], v[144:147]\n"
    "v_exp_f32 v142, v142\n"
    "v_mfma_f32_16x16x32_bf16 v[152:155], a[76:79], a[60:63], v[152:155]\n"
    "buffer_load_dwordx4 v240, s[44:47], s75 offen offset:0 nt lds\n"
    "v_mfma_f32_16x16x32_bf16 v[148:151], a[80:83], a[32:35], v[200:203]\n"
    "v_exp_f32 v143, v143\n"
    "s_nop 3\n"
    "v_mfma_f32_16x16x32_bf16 v[156:159], a[80:83], a[48:51], v[204:207]\n"
    "v_cvt_pk_bf16_f32 v139, v142, v143\n"
    "v_mfma_f32_16x16x32_bf16 v[148:151], a[84:87], a[36:39], v[148:151]\n"
    "buffer_load_dwordx4 v241, s[44:47], s75 offen offset:1024 nt lds\n"
    "v_mfma_f32_16x16x32_bf16 v[156:159], a[84:87], a[52:55], v[156:159]\n"
    "s_waitcnt vmcnt(11)\n"
    "s_barrier\n"
    "v_mfma_f32_16x16x32_bf16 v[148:151], a[88:91], a[40:43], v[148:151]\n"
    "v_exp_f32 v144, v144\n"
    "s_nop 3\n"
    "v_mfma_f32_16x16x32_bf16 v[156:159], a[88:91], a[56:59], v[156:159]\n"
    "ds_read_b128 a[64:67], v224 offset:0\n"
    "ds_read_b128 a[68:71], v225 offset:0\n"
    "v_mfma_f32_16x16x32_bf16 v[148:151], a[92:95], a[44:47], v[148:151]\n"
    "v_exp_f32 v145, v145\n"
    "v_mfma_f32_16x16x32_bf16 v[156:159], a[92:95], a[60:63], v[156:159]\n"
    "s_waitcnt lgkmcnt(2)\n"
    "v_cvt_pk_bf16_f32 v144, v144, v145\n"
    "v_mfma_f32_16x16x32_bf16 v[0:3], a[96:99], v[128:131], v[0:3]\n"
    "v_exp_f32 v146, v146\n"
    "s_nop 3\n"
    "v_mfma_f32_16x16x32_bf16 v[32:35], a[96:99], v[136:139], v[32:35]\n"
    "ds_read_b128 a[72:75], v226 offset:0\n"
    "ds_read_b128 a[76:79], v227 offset:0\n"
    "v_mfma_f32_16x16x32_bf16 v[4:7], a[100:103], v[128:131], v[4:7]\n"
    "v_exp_f32 v147, v147\n"
    "v_mfma_f32_16x16x32_bf16 v[36:39], a[100:103], v[136:139], v[36:39]\n"
    "ds_read_b128 a[80:83], v224 offset:4096\n"
    "v_cvt_pk_bf16_f32 v145, v146, v147\n"
    "v_mfma_f32_16x16x32_bf16 v[8:11], a[104:107], v[128:131], v[8:11]\n"
    "v_exp_f32 v152, v152\n"
    "s_nop 3\n"
    "v_mfma_f32_16x16x32_bf16 v[40:43], a[104:107], v[136:139], v[40:43]\n"
    "ds_read_b128 a[84:87], v225 offset:4096\n"
    "ds_read_b128 a[88:91], v226 offset:4096\n"
    "v_mfma_f32_16x16x32_bf16 v[12:15], a[108:111], v[128:131], v[12:15]\n"
    "v_exp_f32 v153, v153\n"
    "v_mfma_f32_16x16x32_bf16 v[44:47], a[108:111], v[136:139], v[44:47]\n"
    "ds_read_b128 a[92:95], v227 offset:4096\n"
    "v_cvt_pk_bf16_f32 v152, v152, v153\n"
    "v_mfma_f32_16x16x32_bf16 v[16:19], a[112:115], v[128:131], v[16:19]\n"
    "v_exp_f32 v154, v154\n"
    "s_nop 3\n"
    "v_mfma_f32_16x16x32_bf16 v[48:51], a[112:115], v[136:139], v[48:51]\n"
    "buffer_load_dwordx4 v240, s[44:47], s75 offen offset:2048 nt lds\n"
    "v_mfma_f32_16x16x32_bf16 v[20:23], a[116:119], v[128:131], v[20:23]\n"
    "v_exp_f32 v155, v155\n"
    "v_mfma_f32_16x16x32_bf16 v[52:55], a[116:119], v[136:139], v[52:55]\n"
    "v_cvt_pk_bf16_f32 v153, v154, v155\n"
    "v_mfma_f32_16x16x32_bf16 v[24:27], a[120:123], v[128:131], v[24:27]\n"
    "v_exp_f32 v148, v148\n"
    "s_nop 3\n"
    "v_mfma_f32_16x16x32_bf16 v[56:59], a[120:123], v[136:139], v[56:59]\n"
    "buffer_load_dwordx4 v241, s[44:47], s75 offen offset:3072 nt lds\n"
    "v_mfma_f32_16x16x32_bf16 v[28:31], a[124:127], v[128:131], v[28:31]\n"
    "v_exp_f32 v149, v149\n"
    "v_mfma_f32_16x16x32_bf16 v[60:63], a[124:127], v[136:139], v[60:63]\n"
    "v_cvt_pk_bf16_f32 v146, v148, v149\n"
    "v_mfma_f32_16x16x32_bf16 v[208:211], a[128:131], v[128:131], v[208:211]\n"
    "v_exp_f32 v150, v150\n"
    "s_nop 3\n"
    "v_mfma_f32_16x16x32_bf16 v[212:215], a[128:131], v[136:139], v[212:215]\n"
    "s_waitcnt lgkmcnt(0)\n"
    "v_mfma_f32_16x16x32_bf16 v[128:131], a[64:67], a[0:3], v[192:195]\n"
    "v_exp_f32 v151, v151\n"
    "v_mfma_f32_16x16x32_bf16 v[136:139], a[64:67], a[16:19], v[196:199]\n"
    "s_mov_b32 m0, s97\n"
    "v_cvt_pk_bf16_f32 v147, v150, v151\n"
    "v_mfma_f32_16x16x32_bf16 v[128:131], a[68:71], a[4:7], v[128:131]\n"
    "v_exp_f32 v156, v156\n"
    "s_nop 3\n"
    "v_mfma_f32_16x16x32_bf16 v[136:139], a[68:71], a[20:23], v[136:139]\n"
    "buffer_load_dwordx4 v236, s[40:43], s54 offen offset:0 nt lds\n"
    "v_xor_b32 v228, 0x8000, v228\n"
    "v_xor_b32 v229, 0x8000, v229\n"
    "v_mfma_f32_16x16x32_bf16 v[128:131], a[72:75], a[8:11], v[128:131]\n"
    "v_exp_f32 v157, v157\n"
    "v_mfma_f32_16x16x32_bf16 v[136:139], a[72:75], a[24:27], v[136:139]\n"
    "v_cvt_pk_bf16_f32 v154, v156, v157\n"
    "v_mfma_f32_16x16x32_bf16 v[128:131], a[76:79], a[12:15], v[128:131]\n"
    "v_exp_f32 v158, v158\n"
    "s_nop 3\n"
    "v_mfma_f32_16x16x32_bf16 v[136:139], a[76:79], a[28:31], v[136:139]\n"
    "buffer_load_dwordx4 v237, s[40:43], s54 offen offset:1024 nt lds\n"
    "v_xor_b32 v230, 0x8000, v230\n"
    "v_xor_b32 v231, 0x8000, v231\n"
    "v_mfma_f32_16x16x32_bf16 v[132:135], a[80:83], a[0:3], v[192:195]\n"
    "v_exp_f32 v159, v159\n"
    "v_mfma_f32_16x16x32_bf16 v[140:143], a[80:83], a[16:19], v[196:199]\n"
    "v_cvt_pk_bf16_f32 v155, v158, v159\n"
    "v_mfma_f32_16x16x32_bf16 v[132:135], a[84:87], a[4:7], v[132:135]\n"
    "v_xor_b32 v232, 0x8000, v232\n"
    "v_xor_b32 v233, 0x8000, v233\n"
    "v_mfma_f32_16x16x32_bf16 v[140:143], a[84:87], a[20:23], v[140:143]\n"
    "buffer_load_dwordx4 v238, s[40:43], s54 offen offset:2048 nt lds\n"
    "v_xor_b32 v234, 0x8000, v234\n"
    "v_xor_b32 v235, 0x8000, v235\n"
    "v_mfma_f32_16x16x32_bf16 v[132:135], a[88:91], a[8:11], v[132:135]\n"
    "v_exp_f32 v128, v128\n"
    "s_nop 3\n"
    "v_mfma_f32_16x16x32_bf16 v[140:143], a[88:91], a[24:27], v[140:143]\n"
    "buffer_load_dwordx4 v239, s[40:43], s54 offen offset:3072 nt lds\n"
    "v_mfma_f32_16x16x32_bf16 v[132:135], a[92:95], a[12:15], v[132:135]\n"
    "v_exp_f32 v129, v129\n"
    "v_mfma_f32_16x16x32_bf16 v[140:143], a[92:95], a[28:31], v[140:143]\n"
    "s_add_u32 m0, s97, 65536\n"
    "v_cvt_pk_bf16_f32 v128, v128, v129\n"
    "v_mfma_f32_16x16x32_bf16 v[64:67], a[96:99], v[144:147], v[64:67]\n"
    "v_exp_f32 v130, v130\n"
    "s_nop 3\n"
    "v_mfma_f32_16x16x32_bf16 v[96:99], a[96:99], v[152:155], v[96:99]\n"
    "s_waitcnt vmcnt(13)\n"
    "s_barrier\n"
    "v_mfma_f32_16x16x32_bf16 v[68:71], a[100:103], v[144:147], v[68:71]\n"
    "v_exp_f32 v131, v131\n"
    "v_mfma_f32_16x16x32_bf16 v[100:103], a[100:103], v[152:155], v[100:103]\n"
    "ds_read_b64_tr_b16 a[96:97], v228 offset:0\n"
    "v_cvt_pk_bf16_f32 v129, v130, v131\n"
    "v_mfma_f32_16x16x32_bf16 v[72:75], a[104:107], v[144:147], v[72:75]\n"
    "v_exp_f32 v136, v136\n"
    "s_nop 3\n"
    "v_mfma_f32_16x16x32_bf16 v[104:107], a[104:107], v[152:155], v[104:107]\n"
    "ds_read_b64_tr_b16 a[98:99], v228 offset:4096\n"
    "ds_read_b64_tr_b16 a[100:101], v229 offset:0\n"
    "v_mfma_f32_16x16x32_bf16 v[76:79], a[108:111], v[144:147], v[76:79]\n"
    "v_exp_f32 v137, v137\n"
    "v_mfma_f32_16x16x32_bf16 v[108:111], a[108:111], v[152:155], v[108:111]\n"
    "ds_read_b64_tr_b16 a[102:103], v229 offset:4096\n"
    "v_cvt_pk_bf16_f32 v136, v136, v137\n"
    "v_mfma_f32_16x16x32_bf16 v[80:83], a[112:115], v[144:147], v[80:83]\n"
    "v_exp_f32 v138, v138\n"
    "v_mfma_f32_16x16x32_bf16 v[112:115], a[112:115], v[152:155], v[112:115]\n"
    "ds_read_b64_tr_b16 a[104:105], v230 offset:0\n"
    "ds_read_b64_tr_b16 a[106:107], v230 offset:4096\n"
    "v_mfma_f32_16x16x32_bf16 v[84:87], a[116:119], v[144:147], v[84:87]\n"
    "v_exp_f32 v139, v139\n"
    "s_nop 3\n"
    "v_mfma_f32_16x16x32_bf16 v[116:119], a[116:119], v[152:155], v[116:119]\n"
    "ds_read_b64_tr_b16 a[108:109], v231 offset:0\n"
    "v_cvt_pk_bf16_f32 v137, v138, v139\n"
    "v_mfma_f32_16x16x32_bf16 v[88:91], a[120:123], v[144:147], v[88:91]\n"
    "v_exp_f32 v132, v132\n"
    "v_mfma_f32_16x16x32_bf16 v[120:123], a[120:123], v[152:155], v[120:123]\n"
    "ds_read_b64_tr_b16 a[110:111], v231 offset:4096\n"
    "ds_read_b64_tr_b16 a[112:113], v232 offset:0\n"
    "v_mfma_f32_16x16x32_bf16 v[92:95], a[124:127], v[144:147], v[92:95]\n"
    "v_exp_f32 v133, v133\n"
    "s_nop 3\n"
    "v_mfma_f32_16x16x32_bf16 v[124:127], a[124:127], v[152:155], v[124:127]\n"
    "ds_read_b64_tr_b16 a[114:115], v232 offset:4096\n"
    "v_cvt_pk_bf16_f32 v130, v132, v133\n"
    "v_mfma_f32_16x16x32_bf16 v[216:219], a[128:131], v[144:147], v[216:219]\n"
    "v_exp_f32 v134, v134\n"
    "v_mfma_f32_16x16x32_bf16 v[220:223], a[128:131], v[152:155], v[220:223]\n"
    "ds_read_b64_tr_b16 a[116:117], v233 offset:0\n"
    "ds_read_b64_tr_b16 a[118:119], v233 offset:4096\n"
    "v_mfma_f32_16x16x32_bf16 v[144:147], a[64:67], a[32:35], v[200:203]\n"
    "v_exp_f32 v135, v135\n"
    "v_mfma_f32_16x16x32_bf16 v[152:155], a[64:67], a[48:51], v[204:207]\n"
    "ds_read_b64_tr_b16 a[120:121], v234 offset:0\n"
    "v_cvt_pk_bf16_f32 v131, v134, v135\n"
    "v_mfma_f32_16x16x32_bf16 v[144:147], a[68:71], a[36:39], v[144:147]\n"
    "v_exp_f32 v140, v140\n"
    "s_nop 3\n"
    "v_mfma_f32_16x16x32_bf16 v[152:155], a[68:71], a[52:55], v[152:155]\n"
    "ds_read_b64_tr_b16 a[122:123], v234 offset:4096\n"
    "ds_read_b64_tr_b16 a[124:125], v235 offset:0\n"
    "v_mfma_f32_16x16x32_bf16 v[144:147], a[72:75], a[40:43], v[144:147]\n"
    "v_exp_f32 v141, v141\n"
    "v_mfma_f32_16x16x32_bf16 v[152:155], a[72:75], a[56:59], v[152:155]\n"
    "ds_read_b64_tr_b16 a[126:127], v235 offset:4096\n"
    "v_cvt_pk_bf16_f32 v138, v140, v141\n"
    "v_mfma_f32_16x16x32_bf16 v[144:147], a[76:79], a[44:47], v[144:147]\n"
    "v_exp_f32 v142, v142\n"
    "s_nop 3\n"
    "v_mfma_f32_16x16x32_bf16 v[152:155], a[76:79], a[60:63], v[152:155]\n"
    "buffer_load_dwordx4 v240, s[44:47], s54 offen offset:0 nt lds\n"
    "v_mfma_f32_16x16x32_bf16 v[148:151], a[80:83], a[32:35], v[200:203]\n"
    "v_exp_f32 v143, v143\n"
    "v_mfma_f32_16x16x32_bf16 v[156:159], a[80:83], a[48:51], v[204:207]\n"
    "v_cvt_pk_bf16_f32 v139, v142, v143\n"
    "v_mfma_f32_16x16x32_bf16 v[148:151], a[84:87], a[36:39], v[148:151]\n"
    "buffer_load_dwordx4 v241, s[44:47], s54 offen offset:1024 nt lds\n"
    "v_mfma_f32_16x16x32_bf16 v[156:159], a[84:87], a[52:55], v[156:159]\n"
    "v_mfma_f32_16x16x32_bf16 v[148:151], a[88:91], a[40:43], v[148:151]\n"
    "v_exp_f32 v144, v144\n"
    "s_nop 3\n"
    "v_mfma_f32_16x16x32_bf16 v[156:159], a[88:91], a[56:59], v[156:159]\n"
    "ds_read_b128 a[64:67], v224 offset:8192\n"
    "ds_read_b128 a[68:71], v225 offset:8192\n"
    "v_mfma_f32_16x16x32_bf16 v[148:151], a[92:95], a[44:47], v[148:151]\n"
    "v_exp_f32 v145, v145\n"
    "v_mfma_f32_16x16x32_bf16 v[156:159], a[92:95], a[60:63], v[156:159]\n"
    "s_waitcnt lgkmcnt(2)\n"
    "v_cvt_pk_bf16_f32 v144, v144, v145\n"
    "v_mfma_f32_16x16x32_bf16 v[0:3], a[96:99], v[128:131], v[0:3]\n"
    "v_exp_f32 v146, v146\n"
    "s_nop 3\n"
    "v_mfma_f32_16x16x32_bf16 v[32:35], a[96:99], v[136:139], v[32:35]\n"
    "ds_read_b128 a[72:75], v226 offset:8192\n"
    "ds_read_b128 a[76:79], v227 offset:8192\n"
    "v_mfma_f32_16x16x32_bf16 v[4:7], a[100:103], v[128:131], v[4:7]\n"
    "v_exp_f32 v147, v147\n"
    "v_mfma_f32_16x16x32_bf16 v[36:39], a[100:103], v[136:139], v[36:39]\n"
    "ds_read_b128 a[80:83], v224 offset:12288\n"
    "v_cvt_pk_bf16_f32 v145, v146, v147\n"
    "v_mfma_f32_16x16x32_bf16 v[8:11], a[104:107], v[128:131], v[8:11]\n"
    "v_exp_f32 v152, v152\n"
    "s_nop 3\n"
    "v_mfma_f32_16x16x32_bf16 v[40:43], a[104:107], v[136:139], v[40:43]\n"
    "ds_read_b128 a[84:87], v225 offset:12288\n"
    "ds_read_b128 a[88:91], v226 offset:12288\n"
    "v_mfma_f32_16x16x32_bf16 v[12:15], a[108:111], v[128:131], v[12:15]\n"
    "v_exp_f32 v153, v153\n"
    "v_mfma_f32_16x16x32_bf16 v[44:47], a[108:111], v[136:139], v[44:47]\n"
    "ds_read_b128 a[92:95], v227 offset:12288\n"
    "v_cvt_pk_bf16_f32 v152, v152, v153\n"
    "v_mfma_f32_16x16x32_bf16 v[16:19], a[112:115], v[128:131], v[16:19]\n"
    "v_exp_f32 v154, v154\n"
    "v_mfma_f32_16x16x32_bf16 v[48:51], a[112:115], v[136:139], v[48:51]\n"
    "buffer_load_dwordx4 v240, s[44:47], s54 offen offset:2048 nt lds\n"
    "v_mfma_f32_16x16x32_bf16 v[20:23], a[116:119], v[128:131], v[20:23]\n"
    "v_exp_f32 v155, v155\n"
    "s_nop 3\n"
    "v_mfma_f32_16x16x32_bf16 v[52:55], a[116:119], v[136:139], v[52:55]\n"
    "v_cvt_pk_bf16_f32 v153, v154, v155\n"
    "v_mfma_f32_16x16x32_bf16 v[24:27], a[120:123], v[128:131], v[24:27]\n"
    "v_exp_f32 v148, v148\n"
    "v_mfma_f32_16x16x32_bf16 v[56:59], a[120:123], v[136:139], v[56:59]\n"
    "buffer_load_dwordx4 v241, s[44:47], s54 offen offset:3072 nt lds\n"
    "v_mfma_f32_16x16x32_bf16 v[28:31], a[124:127], v[128:131], v[28:31]\n"
    "v_exp_f32 v149, v149\n"
    "s_nop 3\n"
    "v_mfma_f32_16x16x32_bf16 v[60:63], a[124:127], v[136:139], v[60:63]\n"
    "s_add_u32 s54, s54, 0x4000\n"
    "v_cvt_pk_bf16_f32 v146, v148, v149\n"
    "v_mfma_f32_16x16x32_bf16 v[208:211], a[128:131], v[128:131], v[208:211]\n"
    "v_exp_f32 v150, v150\n"
    "v_mfma_f32_16x16x32_bf16 v[212:215], a[128:131], v[136:139], v[212:215]\n"
    "s_waitcnt lgkmcnt(0)\n"
    "v_mfma_f32_16x16x32_bf16 v[128:131], a[64:67], a[0:3], v[192:195]\n"
    "v_exp_f32 v151, v151\n"
    "v_mfma_f32_16x16x32_bf16 v[136:139], a[64:67], a[16:19], v[196:199]\n"
    "s_and_b32 s54, s54, 0x3ffff\n"
    "v_cvt_pk_bf16_f32 v147, v150, v151\n"
    "v_mfma_f32_16x16x32_bf16 v[128:131], a[68:71], a[4:7], v[128:131]\n"
    "v_exp_f32 v156, v156\n"
    "s_nop 3\n"
    "v_mfma_f32_16x16x32_bf16 v[136:139], a[68:71], a[20:23], v[136:139]\n"
    "v_mfma_f32_16x16x32_bf16 v[128:131], a[72:75], a[8:11], v[128:131]\n"
    "v_exp_f32 v157, v157\n"
    "v_mfma_f32_16x16x32_bf16 v[136:139], a[72:75], a[24:27], v[136:139]\n"
    "v_cvt_pk_bf16_f32 v154, v156, v157\n"
    "v_mfma_f32_16x16x32_bf16 v[128:131], a[76:79], a[12:15], v[128:131]\n"
    "v_exp_f32 v158, v158\n"
    "v_mfma_f32_16x16x32_bf16 v[136:139], a[76:79], a[28:31], v[136:139]\n"
    "v_mfma_f32_16x16x32_bf16 v[132:135], a[80:83], a[0:3], v[192:195]\n"
    "v_exp_f32 v159, v159\n"
    "v_mfma_f32_16x16x32_bf16 v[140:143], a[80:83], a[16:19], v[196:199]\n"
    "v_cvt_pk_bf16_f32 v155, v158, v159\n"
    "v_mfma_f32_16x16x32_bf16 v[132:135], a[84:87], a[4:7], v[132:135]\n"
    "s_nop 3\n"
    "v_mfma_f32_16x16x32_bf16 v[140:143], a[84:87], a[20:23], v[140:143]\n"
    "v_mfma_f32_16x16x32_bf16 v[132:135], a[88:91], a[8:11], v[132:135]\n"
    "v_exp_f32 v128, v128\n"
    "v_mfma_f32_16x16x32_bf16 v[140:143], a[88:91], a[24:27], v[140:143]\n"
    "v_mfma_f32_16x16x32_bf16 v[132:135], a[92:95], a[12:15], v[132:135]\n"
    "v_exp_f32 v129, v129\n"
    "v_mfma_f32_16x16x32_bf16 v[140:143], a[92:95], a[28:31], v[140:143]\n"
    "s_add_u32 m0, s97, 16384\n"
    "v_cvt_pk_bf16_f32 v128, v128, v129\n"
    "v_mfma_f32_16x16x32_bf16 v[64:67], a[96:99], v[144:147], v[64:67]\n"
    "v_exp_f32 v130, v130\n"
    "s_nop 3\n"
    "v_mfma_f32_16x16x32_bf16 v[96:99], a[96:99], v[152:155], v[96:99]\n"
    "v_mfma_f32_16x16x32_bf16 v[68:71], a[100:103], v[144:147], v[68:71]\n"
    "v_exp_f32 v131, v131\n"
    "v_mfma_f32_16x16x32_bf16 v[100:103], a[100:103], v[152:155], v[100:103]\n"
    "ds_read_b64_tr_b16 a[96:97], v228 offset:8192\n"
    "v_cvt_pk_bf16_f32 v129, v130, v131\n"
    "v_mfma_f32_16x16x32_bf16 v[72:75], a[104:107], v[144:147], v[72:75]\n"
    "v_exp_f32 v136, v136\n"
    "v_mfma_f32_16x16x32_bf16 v[104:107], a[104:107], v[152:155], v[104:107]\n"
    "ds_read_b64_tr_b16 a[98:99], v228 offset:12288\n"
    "ds_read_b64_tr_b16 a[100:101], v229 offset:8192\n"
    "v_mfma_f32_16x16x32_bf16 v[76:79], a[108:111], v[144:147], v[76:79]\n"
    "v_exp_f32 v137, v137\n"
    "s_nop 3\n"
    "v_mfma_f32_16x16x32_bf16 v[108:111], a[108:111], v[152:155], v[108:111]\n"
    "ds_read_b64_tr_b16 a[102:103], v229 offset:12288\n"
    "v_cvt_pk_bf16_f32 v136, v136, v137\n"
    "v_mfma_f32_16x16x32_bf16 v[80:83], a[112:115], v[144:147], v[80:83]\n"
    "v_exp_f32 v138, v138\n"
    "v_mfma_f32_16x16x32_bf16 v[112:115], a[112:115], v[152:155], v[112:115]\n"
    "ds_read_b64_tr_b16 a[104:105], v230 offset:8192\n"
    "ds_read_b64_tr_b16 a[106:107], v230 offset:12288\n"
    "v_mfma_f32_16x16x32_bf16 v[84:87], a[116:119], v[144:147], v[84:87]\n"
    "v_exp_f32 v139, v139\n"
    "v_mfma_f32_16x16x32_bf16 v[116:119], a[116:119], v[152:155], v[116:119]\n"
    "ds_read_b64_tr_b16 a[108:109], v231 offset:8192\n"
    "v_cvt_pk_bf16_f32 v137, v138, v139\n"
    "v_mfma_f32_16x16x32_bf16 v[88:91], a[120:123], v[144:147], v[88:91]\n"
    "v_exp_f32 v132, v132\n"
    "v_mfma_f32_16x16x32_bf16 v[120:123], a[120:123], v[152:155], v[120:123]\n"
    "ds_read_b64_tr_b16 a[110:111], v231 offset:12288\n"
    "ds_read_b64_tr_b16 a[112:113], v232 offset:8192\n"
    "v_mfma_f32_16x16x32_bf16 v[92:95], a[124:127], v[144:147], v[92:95]\n"
    "v_exp_f32 v133, v133\n"
    "s_nop 3\n"
    "v_mfma_f32_16x16x32_bf16 v[124:127], a[124:127], v[152:155], v[124:127]\n"
    "ds_read_b64_tr_b16 a[114:115], v232 offset:12288\n"
    "v_cvt_pk_bf16_f32 v130, v132, v133\n"
    "v_mfma_f32_16x16x32_bf16 v[216:219], a[128:131], v[144:147], v[216:219]\n"
    "v_exp_f32 v134, v134\n"
    "v_mfma_f32_16x16x32_bf16 v[220:223], a[128:131], v[152:155], v[220:223]\n"
    "ds_read_b64_tr_b16 a[116:117], v233 offset:8192\n"
    "ds_read_b64_tr_b16 a[118:119], v233 offset:12288\n"
    "v_mfma_f32_16x16x32_bf16 v[144:147], a[64:67], a[32:35], v[200:203]\n"
    "v_exp_f32 v135, v135\n"
    "v_mfma_f32_16x16x32_bf16 v[152:155], a[64:67], a[48:51], v[204:207]\n"
    "ds_read_b64_tr_b16 a[120:121], v234 offset:8192\n"
    "v_cvt_pk_bf16_f32 v131, v134, v135\n"
    "v_mfma_f32_16x16x32_bf16 v[144:147], a[68:71], a[36:39], v[144:147]\n"
    "v_exp_f32 v140, v140\n"
    "s_nop 3\n"
    "v_mfma_f32_16x16x32_bf16 v[152:155], a[68:71], a[52:55], v[152:155]\n"
    "ds_read_b64_tr_b16 a[122:123], v234 offset:12288\n"
    "ds_read_b64_tr_b16 a[124:125], v235 offset:8192\n"
    "v_mfma_f32_16x16x32_bf16 v[144:147], a[72:75], a[40:43], v[144:147]\n"
    "v_exp_f32 v141, v141\n"
    "v_mfma_f32_16x16x32_bf16 v[152:155], a[72:75], a[56:59], v[152:155]\n"
    "ds_read_b64_tr_b16 a[126:127], v235 offset:12288\n"
    "v_cvt_pk_bf16_f32 v138, v140, v141\n"
    "v_mfma_f32_16x16x32_bf16 v[144:147], a[76:79], a[44:47], v[144:147]\n"
    "v_exp_f32 v142, v142\n"
    "v_mfma_f32_16x16x32_bf16 v[152:155], a[76:79], a[60:63], v[152:155]\n"
    "buffer_load_dwordx4 v236, s[40:43], s54 offen offset:0 nt lds\n"
    "v_mfma_f32_16x16x32_bf16 v[148:151], a[80:83], a[32:35], v[200:203]\n"
    "v_exp_f32 v143, v143\n"
    "v_mfma_f32_16x16x32_bf16 v[156:159], a[80:83], a[48:51], v[204:207]\n"
    "v_cvt_pk_bf16_f32 v139, v142, v143\n"
    "v_mfma_f32_16x16x32_bf16 v[148:151], a[84:87], a[36:39], v[148:151]\n"
    "buffer_load_dwordx4 v237, s[40:43], s54 offen offset:1024 nt lds\n"
    "s_nop 3\n"
    "v_mfma_f32_16x16x32_bf16 v[156:159], a[84:87], a[52:55], v[156:159]\n"
    "s_waitcnt vmcnt(14)\n"
    "s_barrier\n"
    "v_mfma_f32_16x16x32_bf16 v[148:151], a[88:91], a[40:43], v[148:151]\n"
    "v_exp_f32 v144, v144\n"
    "v_mfma_f32_16x16x32_bf16 v[156:159], a[88:91], a[56:59], v[156:159]\n"
    "ds_read_b128 a[64:67], v224 offset:16384\n"
    "ds_read_b128 a[68:71], v225 offset:16384\n"
    "v_mfma_f32_16x16x32_bf16 v[148:151], a[92:95], a[44:47], v[148:151]\n"
    "v_exp_f32 v145, v145\n"
    "v_mfma_f32_16x16x32_bf16 v[156:159], a[92:95], a[60:63], v[156:159]\n"
    "s_waitcnt lgkmcnt(2)\n"
    "v_cvt_pk_bf16_f32 v144, v144, v145\n"
    "v_mfma_f32_16x16x32_bf16 v[0:3], a[96:99], v[128:131], v[0:3]\n"
    "v_exp_f32 v146, v146\n"
    "s_nop 3\n"
    "v_mfma_f32_16x16x32_bf16 v[32:35], a[96:99], v[136:139], v[32:35]\n"
    "ds_read_b128 a[72:75], v226 offset:16384\n"
    "ds_read_b128 a[76:79], v227 offset:16384\n"
    "v_mfma_f32_16x16x32_bf16 v[4:7], a[100:103], v[128:131], v[4:7]\n"
    "v_exp_f32 v147, v147\n"
    "v_mfma_f32_16x16x32_bf16 v[36:39], a[100:103], v[136:139], v[36:39]\n"
    "ds_read_b128 a[80:83], v224 offset:20480\n"
    "v_cvt_pk_bf16_f32 v145, v146, v147\n"
    "v_mfma_f32_16x16x32_bf16 v[8:11], a[104:107], v[128:131], v[8:11]\n"
    "v_exp_f32 v152, v152\n"
    "v_mfma_f32_16x16x32_bf16 v[40:43], a[104:107], v[136:139], v[40:43]\n"
    "ds_read_b128 a[84:87], v225 offset:20480\n"
    "ds_read_b128 a[88:91], v226 offset:20480\n"
    "v_mfma_f32_16x16x32_bf16 v[12:15], a[108:111], v[128:131], v[12:15]\n"
    "v_exp_f32 v153, v153\n"
    "s_nop 3\n"
    "v_mfma_f32_16x16x32_bf16 v[44:47], a[108:111], v[136:139], v[44:47]\n"
    "ds_read_b128 a[92:95], v227 offset:20480\n"
    "v_cvt_pk_bf16_f32 v152, v152, v153\n"
    "v_mfma_f32_16x16x32_bf16 v[16:19], a[112:115], v[128:131], v[16:19]\n"
    "v_exp_f32 v154, v154\n"
    "v_mfma_f32_16x16x32_bf16 v[48:51], a[112:115], v[136:139], v[48:51]\n"
    "buffer_load_dwordx4 v238, s[40:43], s54 offen offset:2048 nt lds\n"
    "v_mfma_f32_16x16x32_bf16 v[20:23], a[116:119], v[128:131], v[20:23]\n"
    "v_exp_f32 v155, v155\n"
    "v_mfma_f32_16x16x32_bf16 v[52:55], a[116:119], v[136:139], v[52:55]\n"
    "v_cvt_pk_bf16_f32 v153, v154, v155\n"
    "v_mfma_f32_16x16x32_bf16 v[24:27], a[120:123], v[128:131], v[24:27]\n"
    "v_exp_f32 v148, v148\n"
    "v_mfma_f32_16x16x32_bf16 v[56:59], a[120:123], v[136:139], v[56:59]\n"
    "buffer_load_dwordx4 v239, s[40:43], s54 offen offset:3072 nt lds\n"
    "v_mfma_f32_16x16x32_bf16 v[28:31], a[124:127], v[128:131], v[28:31]\n"
    "v_exp_f32 v149, v149\n"
    "s_nop 3\n"
    "v_mfma_f32_16x16x32_bf16 v[60:63], a[124:127], v[136:139], v[60:63]\n"
    "v_cvt_pk_bf16_f32 v146, v148, v149\n"
    "v_mfma_f32_16x16x32_bf16 v[208:211], a[128:131], v[128:131], v[208:211]\n"
    "v_exp_f32 v150, v150\n"
    "v_mfma_f32_16x16x32_bf16 v[212:215], a[128:131], v[136:139], v[212:215]\n"
    "s_waitcnt lgkmcnt(0)\n"
    "v_mfma_f32_16x16x32_bf16 v[128:131], a[64:67], a[0:3], v[192:195]\n"
    "v_exp_f32 v151, v151\n"
    "v_mfma_f32_16x16x32_bf16 v[136:139], a[64:67], a[16:19], v[196:199]\n"
    "v_cvt_pk_bf16_f32 v147, v150, v151\n"
    "v_mfma_f32_16x16x32_bf16 v[128:131], a[68:71], a[4:7], v[128:131]\n"
    "v_exp_f32 v156, v156\n"
    "v_mfma_f32_16x16x32_bf16 v[136:139], a[68:71], a[20:23], v[136:139]\n"
    "v_mfma_f32_16x16x32_bf16 v[128:131], a[72:75], a[8:11], v[128:131]\n"
    "v_exp_f32 v157, v157\n"
    "s_nop 3\n"
    "v_mfma_f32_16x16x32_bf16 v[136:139], a[72:75], a[24:27], v[136:139]\n"
    "v_cvt_pk_bf16_f32 v154, v156, v157\n"
    "v_mfma_f32_16x16x32_bf16 v[128:131], a[76:79], a[12:15], v[128:131]\n"
    "v_exp_f32 v158, v158\n"
    "v_mfma_f32_16x16x32_bf16 v[136:139], a[76:79], a[28:31], v[136:139]\n"
    "v_mfma_f32_16x16x32_bf16 v[132:135], a[80:83], a[0:3], v[192:195]\n"
    "v_exp_f32 v159, v159\n"
    "v_mfma_f32_16x16x32_bf16 v[140:143], a[80:83], a[16:19], v[196:199]\n"
    "v_cvt_pk_bf16_f32 v155, v158, v159\n"
    "v_mfma_f32_16x16x32_bf16 v[132:135], a[84:87], a[4:7], v[132:135]\n"
    "v_mfma_f32_16x16x32_bf16 v[140:143], a[84:87], a[20:23], v[140:143]\n"
    "v_mfma_f32_16x16x32_bf16 v[132:135], a[88:91], a[8:11], v[132:135]\n"
    "v_exp_f32 v128, v128\n"
    "v_mfma_f32_16x16x32_bf16 v[140:143], a[88:91], a[24:27], v[140:143]\n"
    "v_mfma_f32_16x16x32_bf16 v[132:135], a[92:95], a[12:15], v[132:135]\n"
    "v_exp_f32 v129, v129\n"
    "s_nop 3\n"
    "v_mfma_f32_16x16x32_bf16 v[140:143], a[92:95], a[28:31], v[140:143]\n"
    "s_add_u32 m0, s97, 81920\n"
    "v_cvt_pk_bf16_f32 v128, v128, v129\n"
    "v_mfma_f32_16x16x32_bf16 v[64:67], a[96:99], v[144:147], v[64:67]\n"
    "v_exp_f32 v130, v130\n"
    "v_mfma_f32_16x16x32_bf16 v[96:99], a[96:99], v[152:155], v[96:99]\n"
    "s_waitcnt vmcnt(12)\n"
    "s_barrier\n"
    "s_xor_b32 s97, s97, 0x8000\n"
    "v_mfma_f32_16x16x32_bf16 v[68:71], a[100:103], v[144:147], v[68:71]\n"
    "v_exp_f32 v131, v131\n"
    "v_mfma_f32_16x16x32_bf16 v[100:103], a[100:103], v[152:155], v[100:103]\n"
    "ds_read_b64_tr_b16 a[96:97], v228 offset:16384\n"
    "v_cvt_pk_bf16_f32 v129, v130, v131\n"
    "v_mfma_f32_16x16x32_bf16 v[72:75], a[104:107], v[144:147], v[72:75]\n"
    "v_exp_f32 v136, v136\n"
    "v_mfma_f32_16x16x32_bf16 v[104:107], a[104:107], v[152:155], v[104:107]\n"
    "ds_read_b64_tr_b16 a[98:99], v228 offset:20480\n"
    "ds_read_b64_tr_b16 a[100:101], v229 offset:16384\n"
    "v_mfma_f32_16x16x32_bf16 v[76:79], a[108:111], v[144:147], v[76:79]\n"
    "v_exp_f32 v137, v137\n"
    "v_mfma_f32_16x16x32_bf16 v[108:111], a[108:111], v[152:155], v[108:111]\n"
    "ds_read_b64_tr_b16 a[102:103], v229 offset:20480\n"
    "v_cvt_pk_bf16_f32 v136, v136, v137\n"
    "v_mfma_f32_16x16x32_bf16 v[80:83], a[112:115], v[144:147], v[80:83]\n"
    "v_exp_f32 v138, v138\n"
    "s_nop 3\n"
    "v_mfma_f32_16x16x32_bf16 v[112:115], a[112:115], v[152:155], v[112:115]\n"
    "ds_read_b64_tr_b16 a[104:105], v230 offset:16384\n"
    "ds_read_b64_tr_b16 a[106:107], v230 offset:20480\n"
    "v_mfma_f32_16x16x32_bf16 v[84:87], a[116:119], v[144:147], v[84:87]\n"
    "v_exp_f32 v139, v139\n"
    "v_mfma_f32_16x16x32_bf16 v[116:119], a[116:119], v[152:155], v[116:119]\n"
    "ds_read_b64_tr_b16 a[108:109], v231 offset:16384\n"
    "v_cvt_pk_bf16_f32 v137, v138, v139\n"
    "v_mfma_f32_16x16x32_bf16 v[88:91], a[120:123], v[144:147], v[88:91]\n"
    "v_exp_f32 v132, v132\n"
    "v_mfma_f32_16x16x32_bf16 v[120:123], a[120:123], v[152:155], v[120:123]\n"
    "ds_read_b64_tr_b16 a[110:111], v231 offset:20480\n"
    "ds_read_b64_tr_b16 a[112:113], v232 offset:16384\n"
    "v_mfma_f32_16x16x32_bf16 v[92:95], a[124:127], v[144:147], v[92:95]\n"
    "v_exp_f32 v133, v133\n"
    "v_mfma_f32_16x16x32_bf16 v[124:127], a[124:127], v[152:155], v[124:127]\n"
    "ds_read_b64_tr_b16 a[114:115], v232 offset:20480\n"
    "v_cvt_pk_bf16_f32 v130, v132, v133\n"
    "v_mfma_f32_16x16x32_bf16 v[216:219], a[128:131], v[144:147], v[216:219]\n"
    "v_exp_f32 v134, v134\n"
    "v_mfma_f32_16x16x32_bf16 v[220:223], a[128:131], v[152:155], v[220:223]\n"
    "ds_read_b64_tr_b16 a[116:117], v233 offset:16384\n"
    "ds_read_b64_tr_b16 a[118:119], v233 offset:20480\n"
    "v_mfma_f32_16x16x32_bf16 v[144:147], a[64:67], a[32:35], v[200:203]\n"
    "v_exp_f32 v135, v135\n"
    "s_nop 3\n"
    "v_mfma_f32_16x16x32_bf16 v[152:155], a[64:67], a[48:51], v[204:207]\n"
    "ds_read_b64_tr_b16 a[120:121], v234 offset:16384\n"
    "v_cvt_pk_bf16_f32 v131, v134, v135\n"
    "v_mfma_f32_16x16x32_bf16 v[144:147], a[68:71], a[36:39], v[144:147]\n"
    "v_exp_f32 v140, v140\n"
    "v_mfma_f32_16x16x32_bf16 v[152:155], a[68:71], a[52:55], v[152:155]\n"
    "ds_read_b64_tr_b16 a[122:123], v234 offset:20480\n"
    "ds_read_b64_tr_b16 a[124:125], v235 offset:16384\n"
    "v_mfma_f32_16x16x32_bf16 v[144:147], a[72:75], a[40:43], v[144:147]\n"
    "v_exp_f32 v141, v141\n"
    "v_mfma_f32_16x16x32_bf16 v[152:155], a[72:75], a[56:59], v[152:155]\n"
    "ds_read_b64_tr_b16 a[126:127], v235 offset:20480\n"
    "v_cvt_pk_bf16_f32 v138, v140, v141\n"
    "v_mfma_f32_16x16x32_bf16 v[144:147], a[76:79], a[44:47], v[144:147]\n"
    "v_exp_f32 v142, v142\n"
    "v_mfma_f32_16x16x32_bf16 v[152:155], a[76:79], a[60:63], v[152:155]\n"
    "buffer_load_dwordx4 v240, s[44:47], s54 offen offset:0 nt lds\n"
    "v_mfma_f32_16x16x32_bf16 v[148:151], a[80:83], a[32:35], v[200:203]\n"
    "v_exp_f32 v143, v143\n"
    "s_nop 3\n"
    "v_mfma_f32_16x16x32_bf16 v[156:159], a[80:83], a[48:51], v[204:207]\n"
    "v_cvt_pk_bf16_f32 v139, v142, v143\n"
    "v_mfma_f32_16x16x32_bf16 v[148:151], a[84:87], a[36:39], v[148:151]\n"
    "buffer_load_dwordx4 v241, s[44:47], s54 offen offset:1024 nt lds\n"
    "v_mfma_f32_16x16x32_bf16 v[156:159], a[84:87], a[52:55], v[156:159]\n"
    "v_mfma_f32_16x16x32_bf16 v[148:151], a[88:91], a[40:43], v[148:151]\n"
    "v_exp_f32 v144, v144\n"
    "v_mfma_f32_16x16x32_bf16 v[156:159], a[88:91], a[56:59], v[156:159]\n"
    "ds_read_b128 a[64:67], v224 offset:24576\n"
    "ds_read_b128 a[68:71], v225 offset:24576\n"
    "v_mfma_f32_16x16x32_bf16 v[148:151], a[92:95], a[44:47], v[148:151]\n"
    "v_exp_f32 v145, v145\n"
    "v_mfma_f32_16x16x32_bf16 v[156:159], a[92:95], a[60:63], v[156:159]\n"
    "s_waitcnt lgkmcnt(2)\n"
    "v_cvt_pk_bf16_f32 v144, v144, v145\n"
    "v_mfma_f32_16x16x32_bf16 v[0:3], a[96:99], v[128:131], v[0:3]\n"
    "v_exp_f32 v146, v146\n"
    "v_mfma_f32_16x16x32_bf16 v[32:35], a[96:99], v[136:139], v[32:35]\n"
    "ds_read_b128 a[72:75], v226 offset:24576\n"
    "ds_read_b128 a[76:79], v227 offset:24576\n"
    "v_mfma_f32_16x16x32_bf16 v[4:7], a[100:103], v[128:131], v[4:7]\n"
    "v_exp_f32 v147, v147\n"
    "s_nop 3\n"
    "v_mfma_f32_16x16x32_bf16 v[36:39], a[100:103], v[136:139], v[36:39]\n"
    "ds_read_b128 a[80:83], v224 offset:28672\n"
    "v_cvt_pk_bf16_f32 v145, v146, v147\n"
    "v_mfma_f32_16x16x32_bf16 v[8:11], a[104:107], v[128:131], v[8:11]\n"
    "v_exp_f32 v152, v152\n"
    "v_mfma_f32_16x16x32_bf16 v[40:43], a[104:107], v[136:139], v[40:43]\n"
    "ds_read_b128 a[84:87], v225 offset:28672\n"
    "ds_read_b128 a[88:91], v226 offset:28672\n"
    "v_mfma_f32_16x16x32_bf16 v[12:15], a[108:111], v[128:131], v[12:15]\n"
    "v_exp_f32 v153, v153\n"
    "v_mfma_f32_16x16x32_bf16 v[44:47], a[108:111], v[136:139], v[44:47]\n"
    "ds_read_b128 a[92:95], v227 offset:28672\n"
    "v_cvt_pk_bf16_f32 v152, v152, v153\n"
    "v_mfma_f32_16x16x32_bf16 v[16:19], a[112:115], v[128:131], v[16:19]\n"
    "v_exp_f32 v154, v154\n"
    "v_mfma_f32_16x16x32_bf16 v[48:51], a[112:115], v[136:139], v[48:51]\n"
    "buffer_load_dwordx4 v240, s[44:47], s54 offen offset:2048 nt lds\n"
    "v_mfma_f32_16x16x32_bf16 v[20:23], a[116:119], v[128:131], v[20:23]\n"
    "v_exp_f32 v155, v155\n"
    "v_mfma_f32_16x16x32_bf16 v[52:55], a[116:119], v[136:139], v[52:55]\n"
    "v_cvt_pk_bf16_f32 v153, v154, v155\n"
    "v_mfma_f32_16x16x32_bf16 v[24:27], a[120:123], v[128:131], v[24:27]\n"
    "v_exp_f32 v148, v148\n"
    "s_nop 3\n"
    "v_mfma_f32_16x16x32_bf16 v[56:59], a[120:123], v[136:139], v[56:59]\n"
    "buffer_load_dwordx4 v241, s[44:47], s54 offen offset:3072 nt lds\n"
    "v_mfma_f32_16x16x32_bf16 v[28:31], a[124:127], v[128:131], v[28:31]\n"
    "v_exp_f32 v149, v149\n"
    "v_mfma_f32_16x16x32_bf16 v[60:63], a[124:127], v[136:139], v[60:63]\n"
    "s_add_u32 s54, s54, 0x4000\n"
    "v_cvt_pk_bf16_f32 v146, v148, v149\n"
    "v_mfma_f32_16x16x32_bf16 v[208:211], a[128:131], v[128:131], v[208:211]\n"
    "v_exp_f32 v150, v150\n"
    "v_mfma_f32_16x16x32_bf16 v[212:215], a[128:131], v[136:139], v[212:215]\n"
    "s_waitcnt lgkmcnt(0)\n"
    "v_mfma_f32_16x16x32_bf16 v[128:131], a[64:67], a[0:3], v[192:195]\n"
    "v_exp_f32 v151, v151\n"
    "v_mfma_f32_16x16x32_bf16 v[136:139], a[64:67], a[16:19], v[196:199]\n"
    "s_and_b32 s54, s54, 0x3ffff\n"
    "v_cvt_pk_bf16_f32 v147, v150, v151\n"
    "v_mfma_f32_16x16x32_bf16 v[128:131], a[68:71], a[4:7], v[128:131]\n"
    "v_exp_f32 v156, v156\n"
    "v_mfma_f32_16x16x32_bf16 v[136:139], a[68:71], a[20:23], v[136:139]\n"
    "v_xor_b32 v224, 0x8000, v224\n"
    "v_xor_b32 v225, 0x8000, v225\n"
    "v_mfma_f32_16x16x32_bf16 v[128:131], a[72:75], a[8:11], v[128:131]\n"
    "v_exp_f32 v157, v157\n"
    "v_mfma_f32_16x16x32_bf16 v[136:139], a[72:75], a[24:27], v[136:139]\n"
    "v_cvt_pk_bf16_f32 v154, v156, v157\n"
    "v_mfma_f32_16x16x32_bf16 v[128:131], a[76:79], a[12:15], v[128:131]\n"
    "v_exp_f32 v158, v158\n"
    "v_mfma_f32_16x16x32_bf16 v[136:139], a[76:79], a[28:31], v[136:139]\n"
    "v_xor_b32 v226, 0x8000, v226\n"
    "v_xor_b32 v227, 0x8000, v227\n"
    "v_mfma_f32_16x16x32_bf16 v[132:135], a[80:83], a[0:3], v[192:195]\n"
    "v_exp_f32 v159, v159\n"
    "v_mfma_f32_16x16x32_bf16 v[140:143], a[80:83], a[16:19], v[196:199]\n"
    "v_cvt_pk_bf16_f32 v155, v158, v159\n"
    "v_mfma_f32_16x16x32_bf16 v[132:135], a[84:87], a[4:7], v[132:135]\n"
    "s_nop 3\n"
    "v_mfma_f32_16x16x32_bf16 v[140:143], a[84:87], a[20:23], v[140:143]\n"
    "v_mfma_f32_16x16x32_bf16 v[132:135], a[88:91], a[8:11], v[132:135]\n"
    "v_exp_f32 v128, v128\n"
    "v_mfma_f32_16x16x32_bf16 v[140:143], a[88:91], a[24:27], v[140:143]\n"
    "v_mfma_f32_16x16x32_bf16 v[132:135], a[92:95], a[12:15], v[132:135]\n"
    "v_exp_f32 v129, v129\n"
    "v_mfma_f32_16x16x32_bf16 v[140:143], a[92:95], a[28:31], v[140:143]\n"
    "s_mov_b32 m0, s97\n"
    "v_cvt_pk_bf16_f32 v128, v128, v129\n"
    "v_mfma_f32_16x16x32_bf16 v[64:67], a[96:99], v[144:147], v[64:67]\n"
    "v_exp_f32 v130, v130\n"
    "v_mfma_f32_16x16x32_bf16 v[96:99], a[96:99], v[152:155], v[96:99]\n"
    "v_mfma_f32_16x16x32_bf16 v[68:71], a[100:103], v[144:147], v[68:71]\n"
    "v_exp_f32 v131, v131\n"
    "v_mfma_f32_16x16x32_bf16 v[100:103], a[100:103], v[152:155], v[100:103]\n"
    "ds_read_b64_tr_b16 a[96:97], v228 offset:24576\n"
    "v_cvt_pk_bf16_f32 v129, v130, v131\n"
    "v_mfma_f32_16x16x32_bf16 v[72:75], a[104:107], v[144:147], v[72:75]\n"
    "v_exp_f32 v136, v136\n"
    "v_mfma_f32_16x16x32_bf16 v[104:107], a[104:107], v[152:155], v[104:107]\n"
    "ds_read_b64_tr_b16 a[98:99], v228 offset:28672\n"
    "ds_read_b64_tr_b16 a[100:101], v229 offset:24576\n"
    "v_mfma_f32_16x16x32_bf16 v[76:79], a[108:111], v[144:147], v[76:79]\n"
    "v_exp_f32 v137, v137\n"
    "v_mfma_f32_16x16x32_bf16 v[108:111], a[108:111], v[152:155], v[108:111]\n"
    "ds_read_b64_tr_b16 a[102:103], v229 offset:28672\n"
    "v_cvt_pk_bf16_f32 v136, v136, v137\n"
    "v_mfma_f32_16x16x32_bf16 v[80:83], a[112:115], v[144:147], v[80:83]\n"
    "v_exp_f32 v138, v138\n"
    "v_mfma_f32_16x16x32_bf16 v[112:115], a[112:115], v[152:155], v[112:115]\n"
    "ds_read_b64_tr_b16 a[104:105], v230 offset:24576\n"
    "ds_read_b64_tr_b16 a[106:107], v230 offset:28672\n"
    "v_mfma_f32_16x16x32_bf16 v[84:87], a[116:119], v[144:147], v[84:87]\n"
    "v_exp_f32 v139, v139\n"
    "v_mfma_f32_16x16x32_bf16 v[116:119], a[116:119], v[152:155], v[116:119]\n"
    "ds_read_b64_tr_b16 a[108:109], v231 offset:24576\n"
    "v_cvt_pk_bf16_f32 v137, v138, v139\n"
    "v_mfma_f32_16x16x32_bf16 v[88:91], a[120:123], v[144:147], v[88:91]\n"
    "v_exp_f32 v132, v132\n"
    "v_mfma_f32_16x16x32_bf16 v[120:123], a[120:123], v[152:155], v[120:123]\n"
    "ds_read_b64_tr_b16 a[110:111], v231 offset:28672\n"
    "ds_read_b64_tr_b16 a[112:113], v232 offset:24576\n"
    "v_mfma_f32_16x16x32_bf16 v[92:95], a[124:127], v[144:147], v[92:95]\n"
    "v_exp_f32 v133, v133\n"
    "v_mfma_f32_16x16x32_bf16 v[124:127], a[124:127], v[152:155], v[124:127]\n"
    "ds_read_b64_tr_b16 a[114:115], v232 offset:28672\n"
    "v_cvt_pk_bf16_f32 v130, v132, v133\n"
    "v_mfma_f32_16x16x32_bf16 v[216:219], a[128:131], v[144:147], v[216:219]\n"
    "v_exp_f32 v134, v134\n"
    "v_mfma_f32_16x16x32_bf16 v[220:223], a[128:131], v[152:155], v[220:223]\n"
    "ds_read_b64_tr_b16 a[116:117], v233 offset:24576\n"
    "ds_read_b64_tr_b16 a[118:119], v233 offset:28672\n"
    "v_mfma_f32_16x16x32_bf16 v[144:147], a[64:67], a[32:35], v[200:203]\n"
    "v_exp_f32 v135, v135\n"
    "s_nop 3\n"
    "v_mfma_f32_16x16x32_bf16 v[152:155], a[64:67], a[48:51], v[204:207]\n"
    "ds_read_b64_tr_b16 a[120:121], v234 offset:24576\n"
    "v_cvt_pk_bf16_f32 v131, v134, v135\n"
    "v_mfma_f32_16x16x32_bf16 v[144:147], a[68:71], a[36:39], v[144:147]\n"
    "v_exp_f32 v140, v140\n"
    "v_mfma_f32_16x16x32_bf16 v[152:155], a[68:71], a[52:55], v[152:155]\n"
    "ds_read_b64_tr_b16 a[122:123], v234 offset:28672\n"
    "ds_read_b64_tr_b16 a[124:125], v235 offset:24576\n"
    "v_mfma_f32_16x16x32_bf16 v[144:147], a[72:75], a[40:43], v[144:147]\n"
    "v_exp_f32 v141, v141\n"
    "v_mfma_f32_16x16x32_bf16 v[152:155], a[72:75], a[56:59], v[152:155]\n"
    "ds_read_b64_tr_b16 a[126:127], v235 offset:28672\n"
    "v_cvt_pk_bf16_f32 v138, v140, v141\n"
    "v_mfma_f32_16x16x32_bf16 v[144:147], a[76:79], a[44:47], v[144:147]\n"
    "v_exp_f32 v142, v142\n"
    "v_mfma_f32_16x16x32_bf16 v[152:155], a[76:79], a[60:63], v[152:155]\n"
    "buffer_load_dwordx4 v236, s[40:43], s54 offen offset:0 nt lds\n"
    "v_mfma_f32_16x16x32_bf16 v[148:151], a[80:83], a[32:35], v[200:203]\n"
    "v_exp_f32 v143, v143\n"
    "v_mfma_f32_16x16x32_bf16 v[156:159], a[80:83], a[48:51], v[204:207]\n"
    "v_cvt_pk_bf16_f32 v139, v142, v143\n"
    "v_mfma_f32_16x16x32_bf16 v[148:151], a[84:87], a[36:39], v[148:151]\n"
    "buffer_load_dwordx4 v237, s[40:43], s54 offen offset:1024 nt lds\n"
    "v_mfma_f32_16x16x32_bf16 v[156:159], a[84:87], a[52:55], v[156:159]\n"
    "v_mfma_f32_16x16x32_bf16 v[148:151], a[88:91], a[40:43], v[148:151]\n"
    "v_exp_f32 v144, v144\n"
    "v_mfma_f32_16x16x32_bf16 v[156:159], a[88:91], a[56:59], v[156:159]\n"
    "s_waitcnt vmcnt(10) lgkmcnt(0)\n"
    "s_barrier\n"
    "v_mfma_f32_16x16x32_bf16 v[148:151], a[92:95], a[44:47], v[148:151]\n"
    "v_exp_f32 v145, v145\n"
    "v_mfma_f32_16x16x32_bf16 v[156:159], a[92:95], a[60:63], v[156:159]\n"
    "ds_read_b128 a[64:67], v224 offset:0\n"
    "v_cvt_pk_bf16_f32 v144, v144, v145\n"
    "v_mfma_f32_16x16x32_bf16 v[0:3], a[96:99], v[128:131], v[0:3]\n"
    "v_exp_f32 v146, v146\n"
    "v_mfma_f32_16x16x32_bf16 v[32:35], a[96:99], v[136:139], v[32:35]\n"
    "ds_read_b128 a[68:71], v225 offset:0\n"
    "ds_read_b128 a[72:75], v226 offset:0\n"
    "v_mfma_f32_16x16x32_bf16 v[4:7], a[100:103], v[128:131], v[4:7]\n"
    "v_exp_f32 v147, v147\n"
    "v_mfma_f32_16x16x32_bf16 v[36:39], a[100:103], v[136:139], v[36:39]\n"
    "ds_read_b128 a[76:79], v227 offset:0\n"
    "v_cvt_pk_bf16_f32 v145, v146, v147\n"
    "v_mfma_f32_16x16x32_bf16 v[8:11], a[104:107], v[128:131], v[8:11]\n"
    "v_exp_f32 v152, v152\n"
    "v_mfma_f32_16x16x32_bf16 v[40:43], a[104:107], v[136:139], v[40:43]\n"
    "ds_read_b128 a[80:83], v224 offset:4096\n"
    "ds_read_b128 a[84:87], v225 offset:4096\n"
    "v_mfma_f32_16x16x32_bf16 v[12:15], a[108:111], v[128:131], v[12:15]\n"
    "v_exp_f32 v153, v153\n"
    "s_nop 3\n"
    "v_mfma_f32_16x16x32_bf16 v[44:47], a[108:111], v[136:139], v[44:47]\n"
    "ds_read_b128 a[88:91], v226 offset:4096\n"
    "v_cvt_pk_bf16_f32 v152, v152, v153\n"
    "v_mfma_f32_16x16x32_bf16 v[16:19], a[112:115], v[128:131], v[16:19]\n"
    "v_exp_f32 v154, v154\n"
    "v_mfma_f32_16x16x32_bf16 v[48:51], a[112:115], v[136:139], v[48:51]\n"
    "ds_read_b128 a[92:95], v227 offset:4096\n"
    "buffer_load_dwordx4 v238, s[40:43], s54 offen offset:2048 nt lds\n"
    "v_mfma_f32_16x16x32_bf16 v[20:23], a[116:119], v[128:131], v[20:23]\n"
    "v_exp_f32 v155, v155\n"
    "v_mfma_f32_16x16x32_bf16 v[52:55], a[116:119], v[136:139], v[52:55]\n"
    "v_cvt_pk_bf16_f32 v153, v154, v155\n"
    "v_mfma_f32_16x16x32_bf16 v[24:27], a[120:123], v[128:131], v[24:27]\n"
    "v_exp_f32 v148, v148\n"
    "v_mfma_f32_16x16x32_bf16 v[56:59], a[120:123], v[136:139], v[56:59]\n"
    "buffer_load_dwordx4 v239, s[40:43], s54 offen offset:3072 nt lds\n"
    "v_mfma_f32_16x16x32_bf16 v[28:31], a[124:127], v[128:131], v[28:31]\n"
    "v_exp_f32 v149, v149\n"
    "v_mfma_f32_16x16x32_bf16 v[60:63], a[124:127], v[136:139], v[60:63]\n"
    "v_cvt_pk_bf16_f32 v146, v148, v149\n"
    "v_mfma_f32_16x16x32_bf16 v[208:211], a[128:131], v[128:131], v[208:211]\n"
    "v_exp_f32 v150, v150\n"
    "v_mfma_f32_16x16x32_bf16 v[212:215], a[128:131], v[136:139], v[212:215]\n"
    "s_waitcnt lgkmcnt(4)\n"
    ".Lattn_loop:\n"
    "v_mfma_f32_16x16x32_bf16 v[128:131], a[64:67], a[0:3], v[192:195]\n"
    "v_exp_f32 v151, v151\n"
    "v_mfma_f32_16x16x32_bf16 v[136:139], a[64:67], a[16:19], v[196:199]\n"
    "v_cvt_pk_bf16_f32 v147, v150, v151\n"
    "v_mfma_f32_16x16x32_bf16 v[128:131], a[68:71], a[4:7], v[128:131]\n"
    "v_exp_f32 v156, v156\n"
    "v_mfma_f32_16x16x32_bf16 v[136:139], a[68:71], a[20:23], v[136:139]\n"
    "v_xor_b32 v228, 0x8000, v228\n"
    "v_xor_b32 v229, 0x8000, v229\n"
    "v_mfma_f32_16x16x32_bf16 v[128:131], a[72:75], a[8:11], v[128:131]\n"
    "v_exp_f32 v157, v157\n"
    "v_mfma_f32_16x16x32_bf16 v[136:139], a[72:75], a[24:27], v[136:139]\n"
    "s_waitcnt lgkmcnt(0)\n"
    "v_cvt_pk_bf16_f32 v154, v156, v157\n"
    "v_mfma_f32_16x16x32_bf16 v[128:131], a[76:79], a[12:15], v[128:131]\n"
    "v_exp_f32 v158, v158\n"
    "v_mfma_f32_16x16x32_bf16 v[136:139], a[76:79], a[28:31], v[136:139]\n"
    "v_xor_b32 v230, 0x8000, v230\n"
    "v_xor_b32 v231, 0x8000, v231\n"
    "v_mfma_f32_16x16x32_bf16 v[132:135], a[80:83], a[0:3], v[192:195]\n"
    "v_exp_f32 v159, v159\n"
    "v_mfma_f32_16x16x32_bf16 v[140:143], a[80:83], a[16:19], v[196:199]\n"
    "v_cvt_pk_bf16_f32 v155, v158, v159\n"
    "v_mfma_f32_16x16x32_bf16 v[132:135], a[84:87], a[4:7], v[132:135]\n"
    "v_xor_b32 v232, 0x8000, v232\n"
    "v_xor_b32 v233, 0x8000, v233\n"
    "v_mfma_f32_16x16x32_bf16 v[140:143], a[84:87], a[20:23], v[140:143]\n"
    "v_xor_b32 v234, 0x8000, v234\n"
    "v_xor_b32 v235, 0x8000, v235\n"
    "v_mfma_f32_16x16x32_bf16 v[132:135], a[88:91], a[8:11], v[132:135]\n"
    "v_exp_f32 v128, v128\n"
    "v_mfma_f32_16x16x32_bf16 v[140:143], a[88:91], a[24:27], v[140:143]\n"
    "v_mfma_f32_16x16x32_bf16 v[132:135], a[92:95], a[12:15], v[132:135]\n"
    "v_exp_f32 v129, v129\n"
    "v_mfma_f32_16x16x32_bf16 v[140:143], a[92:95], a[28:31], v[140:143]\n"
    "s_add_u32 m0, s97, 65536\n"
    "v_cvt_pk_bf16_f32 v128, v128, v129\n"
    "v_mfma_f32_16x16x32_bf16 v[64:67], a[96:99], v[144:147], v[64:67]\n"
    "v_exp_f32 v130, v130\n"
    "v_mfma_f32_16x16x32_bf16 v[96:99], a[96:99], v[152:155], v[96:99]\n"
    "v_mfma_f32_16x16x32_bf16 v[68:71], a[100:103], v[144:147], v[68:71]\n"
    "v_exp_f32 v131, v131\n"
    "v_mfma_f32_16x16x32_bf16 v[100:103], a[100:103], v[152:155], v[100:103]\n"
    "ds_read_b64_tr_b16 a[96:97], v228 offset:0\n"
    "v_cvt_pk_bf16_f32 v129, v130, v131\n"
    "v_mfma_f32_16x16x32_bf16 v[72:75], a[104:107], v[144:147], v[72:75]\n"
    "v_exp_f32 v136, v136\n"
    "v_mfma_f32_16x16x32_bf16 v[104:107], a[104:107], v[152:155], v[104:107]\n"
    "ds_read_b64_tr_b16 a[98:99], v228 offset:4096\n"
    "ds_read_b64_tr_b16 a[100:101], v229 offset:0\n"
    "v_mfma_f32_16x16x32_bf16 v[76:79], a[108:111], v[144:147], v[76:79]\n"
    "v_exp_f32 v137, v137\n"
    "v_mfma_f32_16x16x32_bf16 v[108:111], a[108:111], v[152:155], v[108:111]\n"
    "ds_read_b64_tr_b16 a[102:103], v229 offset:4096\n"
    "v_cvt_pk_bf16_f32 v136, v136, v137\n"
    "v_mfma_f32_16x16x32_bf16 v[80:83], a[112:115], v[144:147], v[80:83]\n"
    "v_exp_f32 v138, v138\n"
    "v_mfma_f32_16x16x32_bf16 v[112:115], a[112:115], v[152:155], v[112:115]\n"
    "ds_read_b64_tr_b16 a[104:105], v230 offset:0\n"
    "ds_read_b64_tr_b16 a[106:107], v230 offset:4096\n"
    "v_mfma_f32_16x16x32_bf16 v[84:87], a[116:119], v[144:147], v[84:87]\n"
    "v_exp_f32 v139, v139\n"
    "v_mfma_f32_16x16x32_bf16 v[116:119], a[116:119], v[152:155], v[116:119]\n"
    "ds_read_b64_tr_b16 a[108:109], v231 offset:0\n"
    "v_cvt_pk_bf16_f32 v137, v138, v139\n"
    "v_mfma_f32_16x16x32_bf16 v[88:91], a[120:123], v[144:147], v[88:91]\n"
    "v_exp_f32 v132, v132\n"
    "v_mfma_f32_16x16x32_bf16 v[120:123], a[120:123], v[152:155], v[120:123]\n"
    "ds_read_b64_tr_b16 a[110:111], v231 offset:4096\n"
    "ds_read_b64_tr_b16 a[112:113], v232 offset:0\n"
    "v_mfma_f32_16x16x32_bf16 v[92:95], a[124:127], v[144:147], v[92:95]\n"
    "v_exp_f32 v133, v133\n"
    "v_mfma_f32_16x16x32_bf16 v[124:127], a[124:127], v[152:155], v[124:127]\n"
    "ds_read_b64_tr_b16 a[114:115], v232 offset:4096\n"
    "v_cvt_pk_bf16_f32 v130, v132, v133\n"
    "v_mfma_f32_16x16x32_bf16 v[216:219], a[128:131], v[144:147], v[216:219]\n"
    "v_exp_f32 v134, v134\n"
    "v_mfma_f32_16x16x32_bf16 v[220:223], a[128:131], v[152:155], v[220:223]\n"
    "ds_read_b64_tr_b16 a[116:117], v233 offset:0\n"
    "ds_read_b64_tr_b16 a[118:119], v233 offset:4096\n"
    "v_mfma_f32_16x16x32_bf16 v[144:147], a[64:67], a[32:35], v[200:203]\n"
    "v_exp_f32 v135, v135\n"
    "v_mfma_f32_16x16x32_bf16 v[152:155], a[64:67], a[48:51], v[204:207]\n"
    "ds_read_b64_tr_b16 a[120:121], v234 offset:0\n"
    "v_cvt_pk_bf16_f32 v131, v134, v135\n"
    "v_mfma_f32_16x16x32_bf16 v[144:147], a[68:71], a[36:39], v[144:147]\n"
    "v_exp_f32 v140, v140\n"
    "v_mfma_f32_16x16x32_bf16 v[152:155], a[68:71], a[52:55], v[152:155]\n"
    "ds_read_b64_tr_b16 a[122:123], v234 offset:4096\n"
    "ds_read_b64_tr_b16 a[124:125], v235 offset:0\n"
    "v_mfma_f32_16x16x32_bf16 v[144:147], a[72:75], a[40:43], v[144:147]\n"
    "v_exp_f32 v141, v141\n"
    "v_mfma_f32_16x16x32_bf16 v[152:155], a[72:75], a[56:59], v[152:155]\n"
    "ds_read_b64_tr_b16 a[126:127], v235 offset:4096\n"
    "v_cvt_pk_bf16_f32 v138, v140, v141\n"
    "v_mfma_f32_16x16x32_bf16 v[144:147], a[76:79], a[44:47], v[144:147]\n"
    "v_exp_f32 v142, v142\n"
    "v_mfma_f32_16x16x32_bf16 v[152:155], a[76:79], a[60:63], v[152:155]\n"
    "buffer_load_dwordx4 v240, s[44:47], s54 offen offset:0 nt lds\n"
    "v_mfma_f32_16x16x32_bf16 v[148:151], a[80:83], a[32:35], v[200:203]\n"
    "v_exp_f32 v143, v143\n"
    "v_mfma_f32_16x16x32_bf16 v[156:159], a[80:83], a[48:51], v[204:207]\n"
    "v_cvt_pk_bf16_f32 v139, v142, v143\n"
    "v_mfma_f32_16x16x32_bf16 v[148:151], a[84:87], a[36:39], v[148:151]\n"
    "buffer_load_dwordx4 v241, s[44:47], s54 offen offset:1024 nt lds\n"
    "v_mfma_f32_16x16x32_bf16 v[156:159], a[84:87], a[52:55], v[156:159]\n"
    "v_mfma_f32_16x16x32_bf16 v[148:151], a[88:91], a[40:43], v[148:151]\n"
    "v_exp_f32 v144, v144\n"
    "v_mfma_f32_16x16x32_bf16 v[156:159], a[88:91], a[56:59], v[156:159]\n"
    "ds_read_b128 a[64:67], v224 offset:8192\n"
    "ds_read_b128 a[68:71], v225 offset:8192\n"
    "v_mfma_f32_16x16x32_bf16 v[148:151], a[92:95], a[44:47], v[148:151]\n"
    "v_exp_f32 v145, v145\n"
    "v_mfma_f32_16x16x32_bf16 v[156:159], a[92:95], a[60:63], v[156:159]\n"
    "s_waitcnt lgkmcnt(2)\n"
    "v_cvt_pk_bf16_f32 v144, v144, v145\n"
    "v_mfma_f32_16x16x32_bf16 v[0:3], a[96:99], v[128:131], v[0:3]\n"
    "v_exp_f32 v146, v146\n"
    "v_mfma_f32_16x16x32_bf16 v[32:35], a[96:99], v[136:139], v[32:35]\n"
    "ds_read_b128 a[72:75], v226 offset:8192\n"
    "ds_read_b128 a[76:79], v227 offset:8192\n"
    "v_mfma_f32_16x16x32_bf16 v[4:7], a[100:103], v[128:131], v[4:7]\n"
    "v_exp_f32 v147, v147\n"
    "v_mfma_f32_16x16x32_bf16 v[36:39], a[100:103], v[136:139], v[36:39]\n"
    "ds_read_b128 a[80:83], v224 offset:12288\n"
    "v_cvt_pk_bf16_f32 v145, v146, v147\n"
    "v_mfma_f32_16x16x32_bf16 v[8:11], a[104:107], v[128:131], v[8:11]\n"
    "v_exp_f32 v152, v152\n"
    "v_mfma_f32_16x16x32_bf16 v[40:43], a[104:107], v[136:139], v[40:43]\n"
    "ds_read_b128 a[84:87], v225 offset:12288\n"
    "ds_read_b128 a[88:91], v226 offset:12288\n"
    "v_mfma_f32_16x16x32_bf16 v[12:15], a[108:111], v[128:131], v[12:15]\n"
    "v_exp_f32 v153, v153\n"
    "v_mfma_f32_16x16x32_bf16 v[44:47], a[108:111], v[136:139], v[44:47]\n"
    "ds_read_b128 a[92:95], v227 offset:12288\n"
    "v_cvt_pk_bf16_f32 v152, v152, v153\n"
    "v_mfma_f32_16x16x32_bf16 v[16:19], a[112:115], v[128:131], v[16:19]\n"
    "v_exp_f32 v154, v154\n"
    "v_mfma_f32_16x16x32_bf16 v[48:51], a[112:115], v[136:139], v[48:51]\n"
    "buffer_load_dwordx4 v240, s[44:47], s54 offen offset:2048 nt lds\n"
    "v_mfma_f32_16x16x32_bf16 v[20:23], a[116:119], v[128:131], v[20:23]\n"
    "v_exp_f32 v155, v155\n"
    "v_mfma_f32_16x16x32_bf16 v[52:55], a[116:119], v[136:139], v[52:55]\n"
    "v_cvt_pk_bf16_f32 v153, v154, v155\n"
    "v_mfma_f32_16x16x32_bf16 v[24:27], a[120:123], v[128:131], v[24:27]\n"
    "v_exp_f32 v148, v148\n"
    "v_mfma_f32_16x16x32_bf16 v[56:59], a[120:123], v[136:139], v[56:59]\n"
    "buffer_load_dwordx4 v241, s[44:47], s54 offen offset:3072 nt lds\n"
    "v_mfma_f32_16x16x32_bf16 v[28:31], a[124:127], v[128:131], v[28:31]\n"
    "v_exp_f32 v149, v149\n"
    "v_mfma_f32_16x16x32_bf16 v[60:63], a[124:127], v[136:139], v[60:63]\n"
    "s_add_u32 s54, s54, 0x4000\n"
    "v_cvt_pk_bf16_f32 v146, v148, v149\n"
    "v_mfma_f32_16x16x32_bf16 v[208:211], a[128:131], v[128:131], v[208:211]\n"
    "v_exp_f32 v150, v150\n"
    "v_mfma_f32_16x16x32_bf16 v[212:215], a[128:131], v[136:139], v[212:215]\n"
    "s_waitcnt lgkmcnt(0)\n"
    "v_mfma_f32_16x16x32_bf16 v[128:131], a[64:67], a[0:3], v[192:195]\n"
    "v_exp_f32 v151, v151\n"
    "v_mfma_f32_16x16x32_bf16 v[136:139], a[64:67], a[16:19], v[196:199]\n"
    "s_and_b32 s54, s54, 0x3ffff\n"
    "v_cvt_pk_bf16_f32 v147, v150, v151\n"
    "v_mfma_f32_16x16x32_bf16 v[128:131], a[68:71], a[4:7], v[128:131]\n"
    "v_exp_f32 v156, v156\n"
    "v_mfma_f32_16x16x32_bf16 v[136:139], a[68:71], a[20:23], v[136:139]\n"
    "v_mfma_f32_16x16x32_bf16 v[128:131], a[72:75], a[8:11], v[128:131]\n"
    "v_exp_f32 v157, v157\n"
    "v_mfma_f32_16x16x32_bf16 v[136:139], a[72:75], a[24:27], v[136:139]\n"
    "v_cvt_pk_bf16_f32 v154, v156, v157\n"
    "v_mfma_f32_16x16x32_bf16 v[128:131], a[76:79], a[12:15], v[128:131]\n"
    "v_exp_f32 v158, v158\n"
    "v_mfma_f32_16x16x32_bf16 v[136:139], a[76:79], a[28:31], v[136:139]\n"
    "v_mfma_f32_16x16x32_bf16 v[132:135], a[80:83], a[0:3], v[192:195]\n"
    "v_exp_f32 v159, v159\n"
    "v_mfma_f32_16x16x32_bf16 v[140:143], a[80:83], a[16:19], v[196:199]\n"
    "v_cvt_pk_bf16_f32 v155, v158, v159\n"
    "v_mfma_f32_16x16x32_bf16 v[132:135], a[84:87], a[4:7], v[132:135]\n"
    "v_mfma_f32_16x16x32_bf16 v[140:143], a[84:87], a[20:23], v[140:143]\n"
    "v_mfma_f32_16x16x32_bf16 v[132:135], a[88:91], a[8:11], v[132:135]\n"
    "v_exp_f32 v128, v128\n"
    "v_mfma_f32_16x16x32_bf16 v[140:143], a[88:91], a[24:27], v[140:143]\n"
    "v_mfma_f32_16x16x32_bf16 v[132:135], a[92:95], a[12:15], v[132:135]\n"
    "v_exp_f32 v129, v129\n"
    "v_mfma_f32_16x16x32_bf16 v[140:143], a[92:95], a[28:31], v[140:143]\n"
    "s_add_u32 m0, s97, 16384\n"
    "v_cvt_pk_bf16_f32 v128, v128, v129\n"
    "v_mfma_f32_16x16x32_bf16 v[64:67], a[96:99], v[144:147], v[64:67]\n"
    "v_exp_f32 v130, v130\n"
    "v_mfma_f32_16x16x32_bf16 v[96:99], a[96:99], v[152:155], v[96:99]\n"
    "v_mfma_f32_16x16x32_bf16 v[68:71], a[100:103], v[144:147], v[68:71]\n"
    "v_exp_f32 v131, v131\n"
    "v_mfma_f32_16x16x32_bf16 v[100:103], a[100:103], v[152:155], v[100:103]\n"
    "ds_read_b64_tr_b16 a[96:97], v228 offset:8192\n"
    "v_cvt_pk_bf16_f32 v129, v130, v131\n"
    "v_mfma_f32_16x16x32_bf16 v[72:75], a[104:107], v[144:147], v[72:75]\n"
    "v_exp_f32 v136, v136\n"
    "v_mfma_f32_16x16x32_bf16 v[104:107], a[104:107], v[152:155], v[104:107]\n"
    "ds_read_b64_tr_b16 a[98:99], v228 offset:12288\n"
    "ds_read_b64_tr_b16 a[100:101], v229 offset:8192\n"
    "v_mfma_f32_16x16x32_bf16 v[76:79], a[108:111], v[144:147], v[76:79]\n"
    "v_exp_f32 v137, v137\n"
    "v_mfma_f32_16x16x32_bf16 v[108:111], a[108:111], v[152:155], v[108:111]\n"
    "ds_read_b64_tr_b16 a[102:103], v229 offset:12288\n"
    "v_cvt_pk_bf16_f32 v136, v136, v137\n"
    "v_mfma_f32_16x16x32_bf16 v[80:83], a[112:115], v[144:147], v[80:83]\n"
    "v_exp_f32 v138, v138\n"
    "v_mfma_f32_16x16x32_bf16 v[112:115], a[112:115], v[152:155], v[112:115]\n"
    "ds_read_b64_tr_b16 a[104:105], v230 offset:8192\n"
    "ds_read_b64_tr_b16 a[106:107], v230 offset:12288\n"
    "v_mfma_f32_16x16x32_bf16 v[84:87], a[116:119], v[144:147], v[84:87]\n"
    "v_exp_f32 v139, v139\n"
    "v_mfma_f32_16x16x32_bf16 v[116:119], a[116:119], v[152:155], v[116:119]\n"
    "ds_read_b64_tr_b16 a[108:109], v231 offset:8192\n"
    "v_cvt_pk_bf16_f32 v137, v138, v139\n"
    "v_mfma_f32_16x16x32_bf16 v[88:91], a[120:123], v[144:147], v[88:91]\n"
    "v_exp_f32 v132, v132\n"
    "v_mfma_f32_16x16x32_bf16 v[120:123], a[120:123], v[152:155], v[120:123]\n"
    "ds_read_b64_tr_b16 a[110:111], v231 offset:12288\n"
    "ds_read_b64_tr_b16 a[112:113], v232 offset:8192\n"
    "v_mfma_f32_16x16x32_bf16 v[92:95], a[124:127], v[144:147], v[92:95]\n"
    "v_exp_f32 v133, v133\n"
    "v_mfma_f32_16x16x32_bf16 v[124:127], a[124:127], v[152:155], v[124:127]\n"
    "ds_read_b64_tr_b16 a[114:115], v232 offset:12288\n"
    "v_cvt_pk_bf16_f32 v130, v132, v133\n"
    "v_mfma_f32_16x16x32_bf16 v[216:219], a[128:131], v[144:147], v[216:219]\n"
    "v_exp_f32 v134, v134\n"
    "v_mfma_f32_16x16x32_bf16 v[220:223], a[128:131], v[152:155], v[220:223]\n"
    "ds_read_b64_tr_b16 a[116:117], v233 offset:8192\n"
    "ds_read_b64_tr_b16 a[118:119], v233 offset:12288\n"
    "v_mfma_f32_16x16x32_bf16 v[144:147], a[64:67], a[32:35], v[200:203]\n"
    "v_exp_f32 v135, v135\n"
    "v_mfma_f32_16x16x32_bf16 v[152:155], a[64:67], a[48:51], v[204:207]\n"
    "ds_read_b64_tr_b16 a[120:121], v234 offset:8192\n"
    "v_cvt_pk_bf16_f32 v131, v134, v135\n"
    "v_mfma_f32_16x16x32_bf16 v[144:147], a[68:71], a[36:39], v[144:147]\n"
    "v_exp_f32 v140, v140\n"
    "v_mfma_f32_16x16x32_bf16 v[152:155], a[68:71], a[52:55], v[152:155]\n"
    "ds_read_b64_tr_b16 a[122:123], v234 offset:12288\n"
    "ds_read_b64_tr_b16 a[124:125], v235 offset:8192\n"
    "v_mfma_f32_16x16x32_bf16 v[144:147], a[72:75], a[40:43], v[144:147]\n"
    "v_exp_f32 v141, v141\n"
    "v_mfma_f32_16x16x32_bf16 v[152:155], a[72:75], a[56:59], v[152:155]\n"
    "ds_read_b64_tr_b16 a[126:127], v235 offset:12288\n"
    "v_cvt_pk_bf16_f32 v138, v140, v141\n"
    "v_mfma_f32_16x16x32_bf16 v[144:147], a[76:79], a[44:47], v[144:147]\n"
    "v_exp_f32 v142, v142\n"
    "v_mfma_f32_16x16x32_bf16 v[152:155], a[76:79], a[60:63], v[152:155]\n"
    "buffer_load_dwordx4 v236, s[40:43], s54 offen offset:0 nt lds\n"
    "v_mfma_f32_16x16x32_bf16 v[148:151], a[80:83], a[32:35], v[200:203]\n"
    "v_exp_f32 v143, v143\n"
    "v_mfma_f32_16x16x32_bf16 v[156:159], a[80:83], a[48:51], v[204:207]\n"
    "v_cvt_pk_bf16_f32 v139, v142, v143\n"
    "v_mfma_f32_16x16x32_bf16 v[148:151], a[84:87], a[36:39], v[148:151]\n"
    "buffer_load_dwordx4 v237, s[40:43], s54 offen offset:1024 nt lds\n"
    "v_mfma_f32_16x16x32_bf16 v[156:159], a[84:87], a[52:55], v[156:159]\n"
    "v_mfma_f32_16x16x32_bf16 v[148:151], a[88:91], a[40:43], v[148:151]\n"
    "v_exp_f32 v144, v144\n"
    "v_mfma_f32_16x16x32_bf16 v[156:159], a[88:91], a[56:59], v[156:159]\n"
    "s_waitcnt vmcnt(10) lgkmcnt(0)\n"
    "s_barrier\n"
    "v_mfma_f32_16x16x32_bf16 v[148:151], a[92:95], a[44:47], v[148:151]\n"
    "v_exp_f32 v145, v145\n"
    "v_mfma_f32_16x16x32_bf16 v[156:159], a[92:95], a[60:63], v[156:159]\n"
    "ds_read_b128 a[64:67], v224 offset:16384\n"
    "v_cvt_pk_bf16_f32 v144, v144, v145\n"
    "v_mfma_f32_16x16x32_bf16 v[0:3], a[96:99], v[128:131], v[0:3]\n"
    "v_exp_f32 v146, v146\n"
    "v_mfma_f32_16x16x32_bf16 v[32:35], a[96:99], v[136:139], v[32:35]\n"
    "ds_read_b128 a[68:71], v225 offset:16384\n"
    "ds_read_b128 a[72:75], v226 offset:16384\n"
    "v_mfma_f32_16x16x32_bf16 v[4:7], a[100:103], v[128:131], v[4:7]\n"
    "v_exp_f32 v147, v147\n"
    "v_mfma_f32_16x16x32_bf16 v[36:39], a[100:103], v[136:139], v[36:39]\n"
    "ds_read_b128 a[76:79], v227 offset:16384\n"
    "v_cvt_pk_bf16_f32 v145, v146, v147\n"
    "v_mfma_f32_16x16x32_bf16 v[8:11], a[104:107], v[128:131], v[8:11]\n"
    "v_exp_f32 v152, v152\n"
    "v_mfma_f32_16x16x32_bf16 v[40:43], a[104:107], v[136:139], v[40:43]\n"
    "ds_read_b128 a[80:83], v224 offset:20480\n"
    "ds_read_b128 a[84:87], v225 offset:20480\n"
    "v_mfma_f32_16x16x32_bf16 v[12:15], a[108:111], v[128:131], v[12:15]\n"
    "v_exp_f32 v153, v153\n"
    "v_mfma_f32_16x16x32_bf16 v[44:47], a[108:111], v[136:139], v[44:47]\n"
    "ds_read_b128 a[88:91], v226 offset:20480\n"
    "v_cvt_pk_bf16_f32 v152, v152, v153\n"
    "v_mfma_f32_16x16x32_bf16 v[16:19], a[112:115], v[128:131], v[16:19]\n"
    "v_exp_f32 v154, v154\n"
    "v_mfma_f32_16x16x32_bf16 v[48:51], a[112:115], v[136:139], v[48:51]\n"
    "ds_read_b128 a[92:95], v227 offset:20480\n"
    "buffer_load_dwordx4 v238, s[40:43], s54 offen offset:2048 nt lds\n"
    "v_mfma_f32_16x16x32_bf16 v[20:23], a[116:119], v[128:131], v[20:23]\n"
    "v_exp_f32 v155, v155\n"
    "v_mfma_f32_16x16x32_bf16 v[52:55], a[116:119], v[136:139], v[52:55]\n"
    "v_cvt_pk_bf16_f32 v153, v154, v155\n"
    "v_mfma_f32_16x16x32_bf16 v[24:27], a[120:123], v[128:131], v[24:27]\n"
    "v_exp_f32 v148, v148\n"
    "v_mfma_f32_16x16x32_bf16 v[56:59], a[120:123], v[136:139], v[56:59]\n"
    "buffer_load_dwordx4 v239, s[40:43], s54 offen offset:3072 nt lds\n"
    "v_mfma_f32_16x16x32_bf16 v[28:31], a[124:127], v[128:131], v[28:31]\n"
    "v_exp_f32 v149, v149\n"
    "v_mfma_f32_16x16x32_bf16 v[60:63], a[124:127], v[136:139], v[60:63]\n"
    "v_cvt_pk_bf16_f32 v146, v148, v149\n"
    "v_mfma_f32_16x16x32_bf16 v[208:211], a[128:131], v[128:131], v[208:211]\n"
    "v_exp_f32 v150, v150\n"
    "v_mfma_f32_16x16x32_bf16 v[212:215], a[128:131], v[136:139], v[212:215]\n"
    "s_waitcnt lgkmcnt(4)\n"
    "v_mfma_f32_16x16x32_bf16 v[128:131], a[64:67], a[0:3], v[192:195]\n"
    "v_exp_f32 v151, v151\n"
    "v_mfma_f32_16x16x32_bf16 v[136:139], a[64:67], a[16:19], v[196:199]\n"
    "v_cvt_pk_bf16_f32 v147, v150, v151\n"
    "v_mfma_f32_16x16x32_bf16 v[128:131], a[68:71], a[4:7], v[128:131]\n"
    "v_exp_f32 v156, v156\n"
    "v_mfma_f32_16x16x32_bf16 v[136:139], a[68:71], a[20:23], v[136:139]\n"
    "v_mfma_f32_16x16x32_bf16 v[128:131], a[72:75], a[8:11], v[128:131]\n"
    "v_exp_f32 v157, v157\n"
    "v_mfma_f32_16x16x32_bf16 v[136:139], a[72:75], a[24:27], v[136:139]\n"
    "s_waitcnt lgkmcnt(0)\n"
    "v_cvt_pk_bf16_f32 v154, v156, v157\n"
    "v_mfma_f32_16x16x32_bf16 v[128:131], a[76:79], a[12:15], v[128:131]\n"
    "v_exp_f32 v158, v158\n"
    "v_mfma_f32_16x16x32_bf16 v[136:139], a[76:79], a[28:31], v[136:139]\n"
    "v_mfma_f32_16x16x32_bf16 v[132:135], a[80:83], a[0:3], v[192:195]\n"
    "v_exp_f32 v159, v159\n"
    "v_mfma_f32_16x16x32_bf16 v[140:143], a[80:83], a[16:19], v[196:199]\n"
    "v_cvt_pk_bf16_f32 v155, v158, v159\n"
    "v_mfma_f32_16x16x32_bf16 v[132:135], a[84:87], a[4:7], v[132:135]\n"
    "v_mfma_f32_16x16x32_bf16 v[140:143], a[84:87], a[20:23], v[140:143]\n"
    "v_mfma_f32_16x16x32_bf16 v[132:135], a[88:91], a[8:11], v[132:135]\n"
    "v_exp_f32 v128, v128\n"
    "v_mfma_f32_16x16x32_bf16 v[140:143], a[88:91], a[24:27], v[140:143]\n"
    "v_mfma_f32_16x16x32_bf16 v[132:135], a[92:95], a[12:15], v[132:135]\n"
    "v_exp_f32 v129, v129\n"
    "v_mfma_f32_16x16x32_bf16 v[140:143], a[92:95], a[28:31], v[140:143]\n"
    "s_add_u32 m0, s97, 81920\n"
    "v_cvt_pk_bf16_f32 v128, v128, v129\n"
    "v_mfma_f32_16x16x32_bf16 v[64:67], a[96:99], v[144:147], v[64:67]\n"
    "v_exp_f32 v130, v130\n"
    "v_mfma_f32_16x16x32_bf16 v[96:99], a[96:99], v[152:155], v[96:99]\n"
    "s_xor_b32 s97, s97, 0x8000\n"
    "v_mfma_f32_16x16x32_bf16 v[68:71], a[100:103], v[144:147], v[68:71]\n"
    "v_exp_f32 v131, v131\n"
    "v_mfma_f32_16x16x32_bf16 v[100:103], a[100:103], v[152:155], v[100:103]\n"
    "ds_read_b64_tr_b16 a[96:97], v228 offset:16384\n"
    "v_cvt_pk_bf16_f32 v129, v130, v131\n"
    "v_mfma_f32_16x16x32_bf16 v[72:75], a[104:107], v[144:147], v[72:75]\n"
    "v_exp_f32 v136, v136\n"
    "v_mfma_f32_16x16x32_bf16 v[104:107], a[104:107], v[152:155], v[104:107]\n"
    "ds_read_b64_tr_b16 a[98:99], v228 offset:20480\n"
    "ds_read_b64_tr_b16 a[100:101], v229 offset:16384\n"
    "v_mfma_f32_16x16x32_bf16 v[76:79], a[108:111], v[144:147], v[76:79]\n"
    "v_exp_f32 v137, v137\n"
    "v_mfma_f32_16x16x32_bf16 v[108:111], a[108:111], v[152:155], v[108:111]\n"
    "ds_read_b64_tr_b16 a[102:103], v229 offset:20480\n"
    "v_cvt_pk_bf16_f32 v136, v136, v137\n"
    "v_mfma_f32_16x16x32_bf16 v[80:83], a[112:115], v[144:147], v[80:83]\n"
    "v_exp_f32 v138, v138\n"
    "v_mfma_f32_16x16x32_bf16 v[112:115], a[112:115], v[152:155], v[112:115]\n"
    "ds_read_b64_tr_b16 a[104:105], v230 offset:16384\n"
    "ds_read_b64_tr_b16 a[106:107], v230 offset:20480\n"
    "v_mfma_f32_16x16x32_bf16 v[84:87], a[116:119], v[144:147], v[84:87]\n"
    "v_exp_f32 v139, v139\n"
    "v_mfma_f32_16x16x32_bf16 v[116:119], a[116:119], v[152:155], v[116:119]\n"
    "ds_read_b64_tr_b16 a[108:109], v231 offset:16384\n"
    "v_cvt_pk_bf16_f32 v137, v138, v139\n"
    "v_mfma_f32_16x16x32_bf16 v[88:91], a[120:123], v[144:147], v[88:91]\n"
    "v_exp_f32 v132, v132\n"
    "v_mfma_f32_16x16x32_bf16 v[120:123], a[120:123], v[152:155], v[120:123]\n"
    "ds_read_b64_tr_b16 a[110:111], v231 offset:20480\n"
    "ds_read_b64_tr_b16 a[112:113], v232 offset:16384\n"
    "v_mfma_f32_16x16x32_bf16 v[92:95], a[124:127], v[144:147], v[92:95]\n"
    "v_exp_f32 v133, v133\n"
    "v_mfma_f32_16x16x32_bf16 v[124:127], a[124:127], v[152:155], v[124:127]\n"
    "ds_read_b64_tr_b16 a[114:115], v232 offset:20480\n"
    "v_cvt_pk_bf16_f32 v130, v132, v133\n"
    "v_mfma_f32_16x16x32_bf16 v[216:219], a[128:131], v[144:147], v[216:219]\n"
    "v_exp_f32 v134, v134\n"
    "v_mfma_f32_16x16x32_bf16 v[220:223], a[128:131], v[152:155], v[220:223]\n"
    "ds_read_b64_tr_b16 a[116:117], v233 offset:16384\n"
    "ds_read_b64_tr_b16 a[118:119], v233 offset:20480\n"
    "v_mfma_f32_16x16x32_bf16 v[144:147], a[64:67], a[32:35], v[200:203]\n"
    "v_exp_f32 v135, v135\n"
    "v_mfma_f32_16x16x32_bf16 v[152:155], a[64:67], a[48:51], v[204:207]\n"
    "ds_read_b64_tr_b16 a[120:121], v234 offset:16384\n"
    "v_cvt_pk_bf16_f32 v131, v134, v135\n"
    "v_mfma_f32_16x16x32_bf16 v[144:147], a[68:71], a[36:39], v[144:147]\n"
    "v_exp_f32 v140, v140\n"
    "v_mfma_f32_16x16x32_bf16 v[152:155], a[68:71], a[52:55], v[152:155]\n"
    "ds_read_b64_tr_b16 a[122:123], v234 offset:20480\n"
    "ds_read_b64_tr_b16 a[124:125], v235 offset:16384\n"
    "v_mfma_f32_16x16x32_bf16 v[144:147], a[72:75], a[40:43], v[144:147]\n"
    "v_exp_f32 v141, v141\n"
    "v_mfma_f32_16x16x32_bf16 v[152:155], a[72:75], a[56:59], v[152:155]\n"
    "ds_read_b64_tr_b16 a[126:127], v235 offset:20480\n"
    "v_cvt_pk_bf16_f32 v138, v140, v141\n"
    "v_mfma_f32_16x16x32_bf16 v[144:147], a[76:79], a[44:47], v[144:147]\n"
    "v_exp_f32 v142, v142\n"
    "v_mfma_f32_16x16x32_bf16 v[152:155], a[76:79], a[60:63], v[152:155]\n"
    "buffer_load_dwordx4 v240, s[44:47], s54 offen offset:0 nt lds\n"
    "v_mfma_f32_16x16x32_bf16 v[148:151], a[80:83], a[32:35], v[200:203]\n"
    "v_exp_f32 v143, v143\n"
    "v_mfma_f32_16x16x32_bf16 v[156:159], a[80:83], a[48:51], v[204:207]\n"
    "v_cvt_pk_bf16_f32 v139, v142, v143\n"
    "v_mfma_f32_16x16x32_bf16 v[148:151], a[84:87], a[36:39], v[148:151]\n"
    "buffer_load_dwordx4 v241, s[44:47], s54 offen offset:1024 nt lds\n"
    "v_mfma_f32_16x16x32_bf16 v[156:159], a[84:87], a[52:55], v[156:159]\n"
    "v_mfma_f32_16x16x32_bf16 v[148:151], a[88:91], a[40:43], v[148:151]\n"
    "v_exp_f32 v144, v144\n"
    "v_mfma_f32_16x16x32_bf16 v[156:159], a[88:91], a[56:59], v[156:159]\n"
    "ds_read_b128 a[64:67], v224 offset:24576\n"
    "ds_read_b128 a[68:71], v225 offset:24576\n"
    "v_mfma_f32_16x16x32_bf16 v[148:151], a[92:95], a[44:47], v[148:151]\n"
    "v_exp_f32 v145, v145\n"
    "v_mfma_f32_16x16x32_bf16 v[156:159], a[92:95], a[60:63], v[156:159]\n"
    "s_waitcnt lgkmcnt(2)\n"
    "v_cvt_pk_bf16_f32 v144, v144, v145\n"
    "v_mfma_f32_16x16x32_bf16 v[0:3], a[96:99], v[128:131], v[0:3]\n"
    "v_exp_f32 v146, v146\n"
    "v_mfma_f32_16x16x32_bf16 v[32:35], a[96:99], v[136:139], v[32:35]\n"
    "ds_read_b128 a[72:75], v226 offset:24576\n"
    "ds_read_b128 a[76:79], v227 offset:24576\n"
    "v_mfma_f32_16x16x32_bf16 v[4:7], a[100:103], v[128:131], v[4:7]\n"
    "v_exp_f32 v147, v147\n"
    "v_mfma_f32_16x16x32_bf16 v[36:39], a[100:103], v[136:139], v[36:39]\n"
    "ds_read_b128 a[80:83], v224 offset:28672\n"
    "v_cvt_pk_bf16_f32 v145, v146, v147\n"
    "v_mfma_f32_16x16x32_bf16 v[8:11], a[104:107], v[128:131], v[8:11]\n"
    "v_exp_f32 v152, v152\n"
    "v_mfma_f32_16x16x32_bf16 v[40:43], a[104:107], v[136:139], v[40:43]\n"
    "ds_read_b128 a[84:87], v225 offset:28672\n"
    "ds_read_b128 a[88:91], v226 offset:28672\n"
    "v_mfma_f32_16x16x32_bf16 v[12:15], a[108:111], v[128:131], v[12:15]\n"
    "v_exp_f32 v153, v153\n"
    "v_mfma_f32_16x16x32_bf16 v[44:47], a[108:111], v[136:139], v[44:47]\n"
    "ds_read_b128 a[92:95], v227 offset:28672\n"
    "v_cvt_pk_bf16_f32 v152, v152, v153\n"
    "v_mfma_f32_16x16x32_bf16 v[16:19], a[112:115], v[128:131], v[16:19]\n"
    "v_exp_f32 v154, v154\n"
    "v_mfma_f32_16x16x32_bf16 v[48:51], a[112:115], v[136:139], v[48:51]\n"
    "buffer_load_dwordx4 v240, s[44:47], s54 offen offset:2048 nt lds\n"
    "v_mfma_f32_16x16x32_bf16 v[20:23], a[116:119], v[128:131], v[20:23]\n"
    "v_exp_f32 v155, v155\n"
    "v_mfma_f32_16x16x32_bf16 v[52:55], a[116:119], v[136:139], v[52:55]\n"
    "v_cvt_pk_bf16_f32 v153, v154, v155\n"
    "v_mfma_f32_16x16x32_bf16 v[24:27], a[120:123], v[128:131], v[24:27]\n"
    "v_exp_f32 v148, v148\n"
    "v_mfma_f32_16x16x32_bf16 v[56:59], a[120:123], v[136:139], v[56:59]\n"
    "buffer_load_dwordx4 v241, s[44:47], s54 offen offset:3072 nt lds\n"
    "v_mfma_f32_16x16x32_bf16 v[28:31], a[124:127], v[128:131], v[28:31]\n"
    "v_exp_f32 v149, v149\n"
    "v_mfma_f32_16x16x32_bf16 v[60:63], a[124:127], v[136:139], v[60:63]\n"
    "s_add_u32 s54, s54, 0x4000\n"
    "v_cvt_pk_bf16_f32 v146, v148, v149\n"
    "v_mfma_f32_16x16x32_bf16 v[208:211], a[128:131], v[128:131], v[208:211]\n"
    "v_exp_f32 v150, v150\n"
    "v_mfma_f32_16x16x32_bf16 v[212:215], a[128:131], v[136:139], v[212:215]\n"
    "s_waitcnt lgkmcnt(0)\n"
    "v_mfma_f32_16x16x32_bf16 v[128:131], a[64:67], a[0:3], v[192:195]\n"
    "v_exp_f32 v151, v151\n"
    "v_mfma_f32_16x16x32_bf16 v[136:139], a[64:67], a[16:19], v[196:199]\n"
    "s_and_b32 s54, s54, 0x3ffff\n"
    "v_cvt_pk_bf16_f32 v147, v150, v151\n"
    "v_mfma_f32_16x16x32_bf16 v[128:131], a[68:71], a[4:7], v[128:131]\n"
    "v_exp_f32 v156, v156\n"
    "v_mfma_f32_16x16x32_bf16 v[136:139], a[68:71], a[20:23], v[136:139]\n"
    "v_xor_b32 v224, 0x8000, v224\n"
    "v_xor_b32 v225, 0x8000, v225\n"
    "v_mfma_f32_16x16x32_bf16 v[128:131], a[72:75], a[8:11], v[128:131]\n"
    "v_exp_f32 v157, v157\n"
    "v_mfma_f32_16x16x32_bf16 v[136:139], a[72:75], a[24:27], v[136:139]\n"
    "v_cvt_pk_bf16_f32 v154, v156, v157\n"
    "v_mfma_f32_16x16x32_bf16 v[128:131], a[76:79], a[12:15], v[128:131]\n"
    "v_exp_f32 v158, v158\n"
    "v_mfma_f32_16x16x32_bf16 v[136:139], a[76:79], a[28:31], v[136:139]\n"
    "v_xor_b32 v226, 0x8000, v226\n"
    "v_xor_b32 v227, 0x8000, v227\n"
    "v_mfma_f32_16x16x32_bf16 v[132:135], a[80:83], a[0:3], v[192:195]\n"
    "v_exp_f32 v159, v159\n"
    "v_mfma_f32_16x16x32_bf16 v[140:143], a[80:83], a[16:19], v[196:199]\n"
    "v_cvt_pk_bf16_f32 v155, v158, v159\n"
    "v_mfma_f32_16x16x32_bf16 v[132:135], a[84:87], a[4:7], v[132:135]\n"
    "v_mfma_f32_16x16x32_bf16 v[140:143], a[84:87], a[20:23], v[140:143]\n"
    "v_mfma_f32_16x16x32_bf16 v[132:135], a[88:91], a[8:11], v[132:135]\n"
    "v_exp_f32 v128, v128\n"
    "v_mfma_f32_16x16x32_bf16 v[140:143], a[88:91], a[24:27], v[140:143]\n"
    "s_cmp_eq_u32 s98, 0\n"
    "s_cselect_b32 s42, 0, s42\n"
    "v_mfma_f32_16x16x32_bf16 v[132:135], a[92:95], a[12:15], v[132:135]\n"
    "v_exp_f32 v129, v129\n"
    "v_mfma_f32_16x16x32_bf16 v[140:143], a[92:95], a[28:31], v[140:143]\n"
    "s_mov_b32 m0, s97\n"
    "v_cvt_pk_bf16_f32 v128, v128, v129\n"
    "v_mfma_f32_16x16x32_bf16 v[64:67], a[96:99], v[144:147], v[64:67]\n"
    "v_exp_f32 v130, v130\n"
    "v_mfma_f32_16x16x32_bf16 v[96:99], a[96:99], v[152:155], v[96:99]\n"
    "v_mfma_f32_16x16x32_bf16 v[68:71], a[100:103], v[144:147], v[68:71]\n"
    "v_exp_f32 v131, v131\n"
    "v_mfma_f32_16x16x32_bf16 v[100:103], a[100:103], v[152:155], v[100:103]\n"
    "ds_read_b64_tr_b16 a[96:97], v228 offset:24576\n"
    "v_cvt_pk_bf16_f32 v129, v130, v131\n"
    "v_mfma_f32_16x16x32_bf16 v[72:75], a[104:107], v[144:147], v[72:75]\n"
    "v_exp_f32 v136, v136\n"
    "v_mfma_f32_16x16x32_bf16 v[104:107], a[104:107], v[152:155], v[104:107]\n"
    "ds_read_b64_tr_b16 a[98:99], v228 offset:28672\n"
    "ds_read_b64_tr_b16 a[100:101], v229 offset:24576\n"
    "v_mfma_f32_16x16x32_bf16 v[76:79], a[108:111], v[144:147], v[76:79]\n"
    "v_exp_f32 v137, v137\n"
    "v_mfma_f32_16x16x32_bf16 v[108:111], a[108:111], v[152:155], v[108:111]\n"
    "ds_read_b64_tr_b16 a[102:103], v229 offset:28672\n"
    "v_cvt_pk_bf16_f32 v136, v136, v137\n"
    "v_mfma_f32_16x16x32_bf16 v[80:83], a[112:115], v[144:147], v[80:83]\n"
    "v_exp_f32 v138, v138\n"
    "v_mfma_f32_16x16x32_bf16 v[112:115], a[112:115], v[152:155], v[112:115]\n"
    "ds_read_b64_tr_b16 a[104:105], v230 offset:24576\n"
    "ds_read_b64_tr_b16 a[106:107], v230 offset:28672\n"
    "v_mfma_f32_16x16x32_bf16 v[84:87], a[116:119], v[144:147], v[84:87]\n"
    "v_exp_f32 v139, v139\n"
    "v_mfma_f32_16x16x32_bf16 v[116:119], a[116:119], v[152:155], v[116:119]\n"
    "ds_read_b64_tr_b16 a[108:109], v231 offset:24576\n"
    "v_cvt_pk_bf16_f32 v137, v138, v139\n"
    "v_mfma_f32_16x16x32_bf16 v[88:91], a[120:123], v[144:147], v[88:91]\n"
    "v_exp_f32 v132, v132\n"
    "v_mfma_f32_16x16x32_bf16 v[120:123], a[120:123], v[152:155], v[120:123]\n"
    "ds_read_b64_tr_b16 a[110:111], v231 offset:28672\n"
    "ds_read_b64_tr_b16 a[112:113], v232 offset:24576\n"
    "v_mfma_f32_16x16x32_bf16 v[92:95], a[124:127], v[144:147], v[92:95]\n"
    "v_exp_f32 v133, v133\n"
    "v_mfma_f32_16x16x32_bf16 v[124:127], a[124:127], v[152:155], v[124:127]\n"
    "ds_read_b64_tr_b16 a[114:115], v232 offset:28672\n"
    "v_cvt_pk_bf16_f32 v130, v132, v133\n"
    "v_mfma_f32_16x16x32_bf16 v[216:219], a[128:131], v[144:147], v[216:219]\n"
    "v_exp_f32 v134, v134\n"
    "v_mfma_f32_16x16x32_bf16 v[220:223], a[128:131], v[152:155], v[220:223]\n"
    "ds_read_b64_tr_b16 a[116:117], v233 offset:24576\n"
    "ds_read_b64_tr_b16 a[118:119], v233 offset:28672\n"
    "v_mfma_f32_16x16x32_bf16 v[144:147], a[64:67], a[32:35], v[200:203]\n"
    "v_exp_f32 v135, v135\n"
    "v_mfma_f32_16x16x32_bf16 v[152:155], a[64:67], a[48:51], v[204:207]\n"
    "ds_read_b64_tr_b16 a[120:121], v234 offset:24576\n"
    "v_cvt_pk_bf16_f32 v131, v134, v135\n"
    "v_mfma_f32_16x16x32_bf16 v[144:147], a[68:71], a[36:39], v[144:147]\n"
    "v_exp_f32 v140, v140\n"
    "v_mfma_f32_16x16x32_bf16 v[152:155], a[68:71], a[52:55], v[152:155]\n"
    "ds_read_b64_tr_b16 a[122:123], v234 offset:28672\n"
    "ds_read_b64_tr_b16 a[124:125], v235 offset:24576\n"
    "v_mfma_f32_16x16x32_bf16 v[144:147], a[72:75], a[40:43], v[144:147]\n"
    "v_exp_f32 v141, v141\n"
    "v_mfma_f32_16x16x32_bf16 v[152:155], a[72:75], a[56:59], v[152:155]\n"
    "ds_read_b64_tr_b16 a[126:127], v235 offset:28672\n"
    "v_cvt_pk_bf16_f32 v138, v140, v141\n"
    "v_mfma_f32_16x16x32_bf16 v[144:147], a[76:79], a[44:47], v[144:147]\n"
    "v_exp_f32 v142, v142\n"
    "v_mfma_f32_16x16x32_bf16 v[152:155], a[76:79], a[60:63], v[152:155]\n"
    "buffer_load_dwordx4 v236, s[40:43], s54 offen offset:0 nt lds\n"
    "v_mfma_f32_16x16x32_bf16 v[148:151], a[80:83], a[32:35], v[200:203]\n"
    "v_exp_f32 v143, v143\n"
    "v_mfma_f32_16x16x32_bf16 v[156:159], a[80:83], a[48:51], v[204:207]\n"
    "v_cvt_pk_bf16_f32 v139, v142, v143\n"
    "v_mfma_f32_16x16x32_bf16 v[148:151], a[84:87], a[36:39], v[148:151]\n"
    "buffer_load_dwordx4 v237, s[40:43], s54 offen offset:1024 nt lds\n"
    "v_mfma_f32_16x16x32_bf16 v[156:159], a[84:87], a[52:55], v[156:159]\n"
    "v_mfma_f32_16x16x32_bf16 v[148:151], a[88:91], a[40:43], v[148:151]\n"
    "v_exp_f32 v144, v144\n"
    "v_mfma_f32_16x16x32_bf16 v[156:159], a[88:91], a[56:59], v[156:159]\n"
    "s_waitcnt vmcnt(10) lgkmcnt(0)\n"
    "s_barrier\n"
    "v_mfma_f32_16x16x32_bf16 v[148:151], a[92:95], a[44:47], v[148:151]\n"
    "v_exp_f32 v145, v145\n"
    "v_mfma_f32_16x16x32_bf16 v[156:159], a[92:95], a[60:63], v[156:159]\n"
    "ds_read_b128 a[64:67], v224 offset:0\n"
    "v_cvt_pk_bf16_f32 v144, v144, v145\n"
    "v_mfma_f32_16x16x32_bf16 v[0:3], a[96:99], v[128:131], v[0:3]\n"
    "v_exp_f32 v146, v146\n"
    "v_mfma_f32_16x16x32_bf16 v[32:35], a[96:99], v[136:139], v[32:35]\n"
    "ds_read_b128 a[68:71], v225 offset:0\n"
    "ds_read_b128 a[72:75], v226 offset:0\n"
    "v_mfma_f32_16x16x32_bf16 v[4:7], a[100:103], v[128:131], v[4:7]\n"
    "v_exp_f32 v147, v147\n"
    "v_mfma_f32_16x16x32_bf16 v[36:39], a[100:103], v[136:139], v[36:39]\n"
    "ds_read_b128 a[76:79], v227 offset:0\n"
    "v_cvt_pk_bf16_f32 v145, v146, v147\n"
    "v_mfma_f32_16x16x32_bf16 v[8:11], a[104:107], v[128:131], v[8:11]\n"
    "v_exp_f32 v152, v152\n"
    "v_mfma_f32_16x16x32_bf16 v[40:43], a[104:107], v[136:139], v[40:43]\n"
    "ds_read_b128 a[80:83], v224 offset:4096\n"
    "ds_read_b128 a[84:87], v225 offset:4096\n"
    "v_mfma_f32_16x16x32_bf16 v[12:15], a[108:111], v[128:131], v[12:15]\n"
    "v_exp_f32 v153, v153\n"
    "v_mfma_f32_16x16x32_bf16 v[44:47], a[108:111], v[136:139], v[44:47]\n"
    "ds_read_b128 a[88:91], v226 offset:4096\n"
    "v_cvt_pk_bf16_f32 v152, v152, v153\n"
    "v_mfma_f32_16x16x32_bf16 v[16:19], a[112:115], v[128:131], v[16:19]\n"
    "v_exp_f32 v154, v154\n"
    "v_mfma_f32_16x16x32_bf16 v[48:51], a[112:115], v[136:139], v[48:51]\n"
    "ds_read_b128 a[92:95], v227 offset:4096\n"
    "buffer_load_dwordx4 v238, s[40:43], s54 offen offset:2048 nt lds\n"
    "v_mfma_f32_16x16x32_bf16 v[20:23], a[116:119], v[128:131], v[20:23]\n"
    "v_exp_f32 v155, v155\n"
    "v_mfma_f32_16x16x32_bf16 v[52:55], a[116:119], v[136:139], v[52:55]\n"
    "v_cvt_pk_bf16_f32 v153, v154, v155\n"
    "v_mfma_f32_16x16x32_bf16 v[24:27], a[120:123], v[128:131], v[24:27]\n"
    "v_exp_f32 v148, v148\n"
    "v_mfma_f32_16x16x32_bf16 v[56:59], a[120:123], v[136:139], v[56:59]\n"
    "buffer_load_dwordx4 v239, s[40:43], s54 offen offset:3072 nt lds\n"
    "v_mfma_f32_16x16x32_bf16 v[28:31], a[124:127], v[128:131], v[28:31]\n"
    "v_exp_f32 v149, v149\n"
    "v_mfma_f32_16x16x32_bf16 v[60:63], a[124:127], v[136:139], v[60:63]\n"
    "v_cvt_pk_bf16_f32 v146, v148, v149\n"
    "v_mfma_f32_16x16x32_bf16 v[208:211], a[128:131], v[128:131], v[208:211]\n"
    "v_exp_f32 v150, v150\n"
    "v_mfma_f32_16x16x32_bf16 v[212:215], a[128:131], v[136:139], v[212:215]\n"
    "s_waitcnt lgkmcnt(4)\n"
    "s_sub_u32 s98, s98, 1\n"
    "s_cbranch_scc0 .Lattn_loop\n"
    "v_mfma_f32_16x16x32_bf16 v[128:131], a[64:67], a[0:3], v[192:195]\n"
    "v_exp_f32 v151, v151\n"
    "v_mfma_f32_16x16x32_bf16 v[136:139], a[64:67], a[16:19], v[196:199]\n"
    "v_cvt_pk_bf16_f32 v147, v150, v151\n"
    "v_mfma_f32_16x16x32_bf16 v[128:131], a[68:71], a[4:7], v[128:131]\n"
    "v_exp_f32 v156, v156\n"
    "v_mfma_f32_16x16x32_bf16 v[136:139], a[68:71], a[20:23], v[136:139]\n"
    "v_xor_b32 v228, 0x8000, v228\n"
    "v_xor_b32 v229, 0x8000, v229\n"
    "v_mfma_f32_16x16x32_bf16 v[128:131], a[72:75], a[8:11], v[128:131]\n"
    "v_exp_f32 v157, v157\n"
    "v_mfma_f32_16x16x32_bf16 v[136:139], a[72:75], a[24:27], v[136:139]\n"
    "s_waitcnt lgkmcnt(0)\n"
    "v_cvt_pk_bf16_f32 v154, v156, v157\n"
    "v_mfma_f32_16x16x32_bf16 v[128:131], a[76:79], a[12:15], v[128:131]\n"
    "v_exp_f32 v158, v158\n"
    "v_mfma_f32_16x16x32_bf16 v[136:139], a[76:79], a[28:31], v[136:139]\n"
    "v_xor_b32 v230, 0x8000, v230\n"
    "v_xor_b32 v231, 0x8000, v231\n"
    "v_mfma_f32_16x16x32_bf16 v[132:135], a[80:83], a[0:3], v[192:195]\n"
    "v_exp_f32 v159, v159\n"
    "v_mfma_f32_16x16x32_bf16 v[140:143], a[80:83], a[16:19], v[196:199]\n"
    "v_cvt_pk_bf16_f32 v155, v158, v159\n"
    "v_mfma_f32_16x16x32_bf16 v[132:135], a[84:87], a[4:7], v[132:135]\n"
    "v_xor_b32 v232, 0x8000, v232\n"
    "v_xor_b32 v233, 0x8000, v233\n"
    "v_mfma_f32_16x16x32_bf16 v[140:143], a[84:87], a[20:23], v[140:143]\n"
    "v_xor_b32 v234, 0x8000, v234\n"
    "v_xor_b32 v235, 0x8000, v235\n"
    "v_mfma_f32_16x16x32_bf16 v[132:135], a[88:91], a[8:11], v[132:135]\n"
    "v_exp_f32 v128, v128\n"
    "v_mfma_f32_16x16x32_bf16 v[140:143], a[88:91], a[24:27], v[140:143]\n"
    "v_mfma_f32_16x16x32_bf16 v[132:135], a[92:95], a[12:15], v[132:135]\n"
    "v_exp_f32 v129, v129\n"
    "v_mfma_f32_16x16x32_bf16 v[140:143], a[92:95], a[28:31], v[140:143]\n"
    "v_cvt_pk_bf16_f32 v128, v128, v129\n"
    "v_mfma_f32_16x16x32_bf16 v[64:67], a[96:99], v[144:147], v[64:67]\n"
    "v_exp_f32 v130, v130\n"
    "v_mfma_f32_16x16x32_bf16 v[96:99], a[96:99], v[152:155], v[96:99]\n"
    "v_mfma_f32_16x16x32_bf16 v[68:71], a[100:103], v[144:147], v[68:71]\n"
    "v_exp_f32 v131, v131\n"
    "v_mfma_f32_16x16x32_bf16 v[100:103], a[100:103], v[152:155], v[100:103]\n"
    "ds_read_b64_tr_b16 a[96:97], v228 offset:0\n"
    "v_cvt_pk_bf16_f32 v129, v130, v131\n"
    "v_mfma_f32_16x16x32_bf16 v[72:75], a[104:107], v[144:147], v[72:75]\n"
    "v_exp_f32 v136, v136\n"
    "v_mfma_f32_16x16x32_bf16 v[104:107], a[104:107], v[152:155], v[104:107]\n"
    "ds_read_b64_tr_b16 a[98:99], v228 offset:4096\n"
    "ds_read_b64_tr_b16 a[100:101], v229 offset:0\n"
    "v_mfma_f32_16x16x32_bf16 v[76:79], a[108:111], v[144:147], v[76:79]\n"
    "v_exp_f32 v137, v137\n"
    "v_mfma_f32_16x16x32_bf16 v[108:111], a[108:111], v[152:155], v[108:111]\n"
    "ds_read_b64_tr_b16 a[102:103], v229 offset:4096\n"
    "v_cvt_pk_bf16_f32 v136, v136, v137\n"
    "v_mfma_f32_16x16x32_bf16 v[80:83], a[112:115], v[144:147], v[80:83]\n"
    "v_exp_f32 v138, v138\n"
    "v_mfma_f32_16x16x32_bf16 v[112:115], a[112:115], v[152:155], v[112:115]\n"
    "ds_read_b64_tr_b16 a[104:105], v230 offset:0\n"
    "ds_read_b64_tr_b16 a[106:107], v230 offset:4096\n"
    "v_mfma_f32_16x16x32_bf16 v[84:87], a[116:119], v[144:147], v[84:87]\n"
    "v_exp_f32 v139, v139\n"
    "v_mfma_f32_16x16x32_bf16 v[116:119], a[116:119], v[152:155], v[116:119]\n"
    "ds_read_b64_tr_b16 a[108:109], v231 offset:0\n"
    "v_cvt_pk_bf16_f32 v137, v138, v139\n"
    "v_mfma_f32_16x16x32_bf16 v[88:91], a[120:123], v[144:147], v[88:91]\n"
    "v_exp_f32 v132, v132\n"
    "v_mfma_f32_16x16x32_bf16 v[120:123], a[120:123], v[152:155], v[120:123]\n"
    "ds_read_b64_tr_b16 a[110:111], v231 offset:4096\n"
    "ds_read_b64_tr_b16 a[112:113], v232 offset:0\n"
    "v_mfma_f32_16x16x32_bf16 v[92:95], a[124:127], v[144:147], v[92:95]\n"
    "v_exp_f32 v133, v133\n"
    "v_mfma_f32_16x16x32_bf16 v[124:127], a[124:127], v[152:155], v[124:127]\n"
    "ds_read_b64_tr_b16 a[114:115], v232 offset:4096\n"
    "v_cvt_pk_bf16_f32 v130, v132, v133\n"
    "v_mfma_f32_16x16x32_bf16 v[216:219], a[128:131], v[144:147], v[216:219]\n"
    "v_exp_f32 v134, v134\n"
    "v_mfma_f32_16x16x32_bf16 v[220:223], a[128:131], v[152:155], v[220:223]\n"
    "ds_read_b64_tr_b16 a[116:117], v233 offset:0\n"
    "ds_read_b64_tr_b16 a[118:119], v233 offset:4096\n"
    "v_mfma_f32_16x16x32_bf16 v[144:147], a[64:67], a[32:35], v[200:203]\n"
    "v_exp_f32 v135, v135\n"
    "v_mfma_f32_16x16x32_bf16 v[152:155], a[64:67], a[48:51], v[204:207]\n"
    "ds_read_b64_tr_b16 a[120:121], v234 offset:0\n"
    "v_cvt_pk_bf16_f32 v131, v134, v135\n"
    "v_mfma_f32_16x16x32_bf16 v[144:147], a[68:71], a[36:39], v[144:147]\n"
    "v_exp_f32 v140, v140\n"
    "v_mfma_f32_16x16x32_bf16 v[152:155], a[68:71], a[52:55], v[152:155]\n"
    "ds_read_b64_tr_b16 a[122:123], v234 offset:4096\n"
    "ds_read_b64_tr_b16 a[124:125], v235 offset:0\n"
    "v_mfma_f32_16x16x32_bf16 v[144:147], a[72:75], a[40:43], v[144:147]\n"
    "v_exp_f32 v141, v141\n"
    "v_mfma_f32_16x16x32_bf16 v[152:155], a[72:75], a[56:59], v[152:155]\n"
    "ds_read_b64_tr_b16 a[126:127], v235 offset:4096\n"
    "v_cvt_pk_bf16_f32 v138, v140, v141\n"
    "v_mfma_f32_16x16x32_bf16 v[144:147], a[76:79], a[44:47], v[144:147]\n"
    "v_exp_f32 v142, v142\n"
    "v_mfma_f32_16x16x32_bf16 v[152:155], a[76:79], a[60:63], v[152:155]\n"
    "v_mfma_f32_16x16x32_bf16 v[148:151], a[80:83], a[32:35], v[200:203]\n"
    "v_exp_f32 v143, v143\n"
    "v_mfma_f32_16x16x32_bf16 v[156:159], a[80:83], a[48:51], v[204:207]\n"
    "v_cvt_pk_bf16_f32 v139, v142, v143\n"
    "v_mfma_f32_16x16x32_bf16 v[148:151], a[84:87], a[36:39], v[148:151]\n"
    "v_mfma_f32_16x16x32_bf16 v[156:159], a[84:87], a[52:55], v[156:159]\n"
    "v_mfma_f32_16x16x32_bf16 v[148:151], a[88:91], a[40:43], v[148:151]\n"
    "v_exp_f32 v144, v144\n"
    "v_mfma_f32_16x16x32_bf16 v[156:159], a[88:91], a[56:59], v[156:159]\n"
    "ds_read_b128 a[64:67], v224 offset:8192\n"
    "ds_read_b128 a[68:71], v225 offset:8192\n"
    "v_mfma_f32_16x16x32_bf16 v[148:151], a[92:95], a[44:47], v[148:151]\n"
    "v_exp_f32 v145, v145\n"
    "v_mfma_f32_16x16x32_bf16 v[156:159], a[92:95], a[60:63], v[156:159]\n"
    "s_waitcnt lgkmcnt(2)\n"
    "v_cvt_pk_bf16_f32 v144, v144, v145\n"
    "v_mfma_f32_16x16x32_bf16 v[0:3], a[96:99], v[128:131], v[0:3]\n"
    "v_exp_f32 v146, v146\n"
    "v_mfma_f32_16x16x32_bf16 v[32:35], a[96:99], v[136:139], v[32:35]\n"
    "ds_read_b128 a[72:75], v226 offset:8192\n"
    "ds_read_b128 a[76:79], v227 offset:8192\n"
    "v_mfma_f32_16x16x32_bf16 v[4:7], a[100:103], v[128:131], v[4:7]\n"
    "v_exp_f32 v147, v147\n"
    "v_mfma_f32_16x16x32_bf16 v[36:39], a[100:103], v[136:139], v[36:39]\n"
    "ds_read_b128 a[80:83], v224 offset:12288\n"
    "v_cvt_pk_bf16_f32 v145, v146, v147\n"
    "v_mfma_f32_16x16x32_bf16 v[8:11], a[104:107], v[128:131], v[8:11]\n"
    "v_exp_f32 v152, v152\n"
    "v_mfma_f32_16x16x32_bf16 v[40:43], a[104:107], v[136:139], v[40:43]\n"
    "ds_read_b128 a[84:87], v225 offset:12288\n"
    "ds_read_b128 a[88:91], v226 offset:12288\n"
    "v_mfma_f32_16x16x32_bf16 v[12:15], a[108:111], v[128:131], v[12:15]\n"
    "v_exp_f32 v153, v153\n"
    "v_mfma_f32_16x16x32_bf16 v[44:47], a[108:111], v[136:139], v[44:47]\n"
    "ds_read_b128 a[92:95], v227 offset:12288\n"
    "v_cvt_pk_bf16_f32 v152, v152, v153\n"
    "v_mfma_f32_16x16x32_bf16 v[16:19], a[112:115], v[128:131], v[16:19]\n"
    "v_exp_f32 v154, v154\n"
    "v_mfma_f32_16x16x32_bf16 v[48:51], a[112:115], v[136:139], v[48:51]\n"
    "v_mfma_f32_16x16x32_bf16 v[20:23], a[116:119], v[128:131], v[20:23]\n"
    "v_exp_f32 v155, v155\n"
    "v_mfma_f32_16x16x32_bf16 v[52:55], a[116:119], v[136:139], v[52:55]\n"
    "v_cvt_pk_bf16_f32 v153, v154, v155\n"
    "v_mfma_f32_16x16x32_bf16 v[24:27], a[120:123], v[128:131], v[24:27]\n"
    "v_exp_f32 v148, v148\n"
    "v_mfma_f32_16x16x32_bf16 v[56:59], a[120:123], v[136:139], v[56:59]\n"
    "v_mfma_f32_16x16x32_bf16 v[28:31], a[124:127], v[128:131], v[28:31]\n"
    "v_exp_f32 v149, v149\n"
    "v_mfma_f32_16x16x32_bf16 v[60:63], a[124:127], v[136:139], v[60:63]\n"
    "v_cvt_pk_bf16_f32 v146, v148, v149\n"
    "v_mfma_f32_16x16x32_bf16 v[208:211], a[128:131], v[128:131], v[208:211]\n"
    "v_exp_f32 v150, v150\n"
    "v_mfma_f32_16x16x32_bf16 v[212:215], a[128:131], v[136:139], v[212:215]\n"
    "s_waitcnt lgkmcnt(0)\n"
    "v_mfma_f32_16x16x32_bf16 v[128:131], a[64:67], a[0:3], v[192:195]\n"
    "v_exp_f32 v151, v151\n"
    "v_mfma_f32_16x16x32_bf16 v[136:139], a[64:67], a[16:19], v[196:199]\n"
    "v_cvt_pk_bf16_f32 v147, v150, v151\n"
    "v_mfma_f32_16x16x32_bf16 v[128:131], a[68:71], a[4:7], v[128:131]\n"
    "v_exp_f32 v156, v156\n"
    "v_mfma_f32_16x16x32_bf16 v[136:139], a[68:71], a[20:23], v[136:139]\n"
    "v_mfma_f32_16x16x32_bf16 v[128:131], a[72:75], a[8:11], v[128:131]\n"
    "v_exp_f32 v157, v157\n"
    "v_mfma_f32_16x16x32_bf16 v[136:139], a[72:75], a[24:27], v[136:139]\n"
    "v_cvt_pk_bf16_f32 v154, v156, v157\n"
    "v_mfma_f32_16x16x32_bf16 v[128:131], a[76:79], a[12:15], v[128:131]\n"
    "v_exp_f32 v158, v158\n"
    "v_mfma_f32_16x16x32_bf16 v[136:139], a[76:79], a[28:31], v[136:139]\n"
    "v_mfma_f32_16x16x32_bf16 v[132:135], a[80:83], a[0:3], v[192:195]\n"
    "v_exp_f32 v159, v159\n"
    "v_mfma_f32_16x16x32_bf16 v[140:143], a[80:83], a[16:19], v[196:199]\n"
    "v_cvt_pk_bf16_f32 v155, v158, v159\n"
    "v_mfma_f32_16x16x32_bf16 v[132:135], a[84:87], a[4:7], v[132:135]\n"
    "v_mfma_f32_16x16x32_bf16 v[140:143], a[84:87], a[20:23], v[140:143]\n"
    "v_mfma_f32_16x16x32_bf16 v[132:135], a[88:91], a[8:11], v[132:135]\n"
    "v_exp_f32 v128, v128\n"
    "v_mfma_f32_16x16x32_bf16 v[140:143], a[88:91], a[24:27], v[140:143]\n"
    "v_mfma_f32_16x16x32_bf16 v[132:135], a[92:95], a[12:15], v[132:135]\n"
    "v_exp_f32 v129, v129\n"
    "v_mfma_f32_16x16x32_bf16 v[140:143], a[92:95], a[28:31], v[140:143]\n"
    "v_cvt_pk_bf16_f32 v128, v128, v129\n"
    "v_mfma_f32_16x16x32_bf16 v[64:67], a[96:99], v[144:147], v[64:67]\n"
    "v_exp_f32 v130, v130\n"
    "v_mfma_f32_16x16x32_bf16 v[96:99], a[96:99], v[152:155], v[96:99]\n"
    "v_mfma_f32_16x16x32_bf16 v[68:71], a[100:103], v[144:147], v[68:71]\n"
    "v_exp_f32 v131, v131\n"
    "v_mfma_f32_16x16x32_bf16 v[100:103], a[100:103], v[152:155], v[100:103]\n"
    "ds_read_b64_tr_b16 a[96:97], v228 offset:8192\n"
    "v_cvt_pk_bf16_f32 v129, v130, v131\n"
    "v_mfma_f32_16x16x32_bf16 v[72:75], a[104:107], v[144:147], v[72:75]\n"
    "v_exp_f32 v136, v136\n"
    "v_mfma_f32_16x16x32_bf16 v[104:107], a[104:107], v[152:155], v[104:107]\n"
    "ds_read_b64_tr_b16 a[98:99], v228 offset:12288\n"
    "ds_read_b64_tr_b16 a[100:101], v229 offset:8192\n"
    "v_mfma_f32_16x16x32_bf16 v[76:79], a[108:111], v[144:147], v[76:79]\n"
    "v_exp_f32 v137, v137\n"
    "v_mfma_f32_16x16x32_bf16 v[108:111], a[108:111], v[152:155], v[108:111]\n"
    "ds_read_b64_tr_b16 a[102:103], v229 offset:12288\n"
    "v_cvt_pk_bf16_f32 v136, v136, v137\n"
    "v_mfma_f32_16x16x32_bf16 v[80:83], a[112:115], v[144:147], v[80:83]\n"
    "v_exp_f32 v138, v138\n"
    "v_mfma_f32_16x16x32_bf16 v[112:115], a[112:115], v[152:155], v[112:115]\n"
    "ds_read_b64_tr_b16 a[104:105], v230 offset:8192\n"
    "ds_read_b64_tr_b16 a[106:107], v230 offset:12288\n"
    "v_mfma_f32_16x16x32_bf16 v[84:87], a[116:119], v[144:147], v[84:87]\n"
    "v_exp_f32 v139, v139\n"
    "v_mfma_f32_16x16x32_bf16 v[116:119], a[116:119], v[152:155], v[116:119]\n"
    "ds_read_b64_tr_b16 a[108:109], v231 offset:8192\n"
    "v_cvt_pk_bf16_f32 v137, v138, v139\n"
    "v_mfma_f32_16x16x32_bf16 v[88:91], a[120:123], v[144:147], v[88:91]\n"
    "v_exp_f32 v132, v132\n"
    "v_mfma_f32_16x16x32_bf16 v[120:123], a[120:123], v[152:155], v[120:123]\n"
    "ds_read_b64_tr_b16 a[110:111], v231 offset:12288\n"
    "ds_read_b64_tr_b16 a[112:113], v232 offset:8192\n"
    "v_mfma_f32_16x16x32_bf16 v[92:95], a[124:127], v[144:147], v[92:95]\n"
    "v_exp_f32 v133, v133\n"
    "v_mfma_f32_16x16x32_bf16 v[124:127], a[124:127], v[152:155], v[124:127]\n"
    "ds_read_b64_tr_b16 a[114:115], v232 offset:12288\n"
    "v_cvt_pk_bf16_f32 v130, v132, v133\n"
    "v_mfma_f32_16x16x32_bf16 v[216:219], a[128:131], v[144:147], v[216:219]\n"
    "v_exp_f32 v134, v134\n"
    "v_mfma_f32_16x16x32_bf16 v[220:223], a[128:131], v[152:155], v[220:223]\n"
    "ds_read_b64_tr_b16 a[116:117], v233 offset:8192\n"
    "ds_read_b64_tr_b16 a[118:119], v233 offset:12288\n"
    "v_mfma_f32_16x16x32_bf16 v[144:147], a[64:67], a[32:35], v[200:203]\n"
    "v_exp_f32 v135, v135\n"
    "v_mfma_f32_16x16x32_bf16 v[152:155], a[64:67], a[48:51], v[204:207]\n"
    "ds_read_b64_tr_b16 a[120:121], v234 offset:8192\n"
    "v_cvt_pk_bf16_f32 v131, v134, v135\n"
    "v_mfma_f32_16x16x32_bf16 v[144:147], a[68:71], a[36:39], v[144:147]\n"
    "v_exp_f32 v140, v140\n"
    "v_mfma_f32_16x16x32_bf16 v[152:155], a[68:71], a[52:55], v[152:155]\n"
    "ds_read_b64_tr_b16 a[122:123], v234 offset:12288\n"
    "ds_read_b64_tr_b16 a[124:125], v235 offset:8192\n"
    "v_mfma_f32_16x16x32_bf16 v[144:147], a[72:75], a[40:43], v[144:147]\n"
    "v_exp_f32 v141, v141\n"
    "v_mfma_f32_16x16x32_bf16 v[152:155], a[72:75], a[56:59], v[152:155]\n"
    "ds_read_b64_tr_b16 a[126:127], v235 offset:12288\n"
    "v_cvt_pk_bf16_f32 v138, v140, v141\n"
    "v_mfma_f32_16x16x32_bf16 v[144:147], a[76:79], a[44:47], v[144:147]\n"
    "v_exp_f32 v142, v142\n"
    "v_mfma_f32_16x16x32_bf16 v[152:155], a[76:79], a[60:63], v[152:155]\n"
    "v_mfma_f32_16x16x32_bf16 v[148:151], a[80:83], a[32:35], v[200:203]\n"
    "v_exp_f32 v143, v143\n"
    "v_mfma_f32_16x16x32_bf16 v[156:159], a[80:83], a[48:51], v[204:207]\n"
    "v_cvt_pk_bf16_f32 v139, v142, v143\n"
    "v_mfma_f32_16x16x32_bf16 v[148:151], a[84:87], a[36:39], v[148:151]\n"
    "v_mfma_f32_16x16x32_bf16 v[156:159], a[84:87], a[52:55], v[156:159]\n"
    "v_mfma_f32_16x16x32_bf16 v[148:151], a[88:91], a[40:43], v[148:151]\n"
    "v_exp_f32 v144, v144\n"
    "v_mfma_f32_16x16x32_bf16 v[156:159], a[88:91], a[56:59], v[156:159]\n"
    "s_waitcnt vmcnt(4) lgkmcnt(0)\n"
    "s_barrier\n"
    "v_mfma_f32_16x16x32_bf16 v[148:151], a[92:95], a[44:47], v[148:151]\n"
    "v_exp_f32 v145, v145\n"
    "v_mfma_f32_16x16x32_bf16 v[156:159], a[92:95], a[60:63], v[156:159]\n"
    "ds_read_b128 a[64:67], v224 offset:16384\n"
    "v_cvt_pk_bf16_f32 v144, v144, v145\n"
    "v_mfma_f32_16x16x32_bf16 v[0:3], a[96:99], v[128:131], v[0:3]\n"
    "v_exp_f32 v146, v146\n"
    "v_mfma_f32_16x16x32_bf16 v[32:35], a[96:99], v[136:139], v[32:35]\n"
    "ds_read_b128 a[68:71], v225 offset:16384\n"
    "ds_read_b128 a[72:75], v226 offset:16384\n"
    "v_mfma_f32_16x16x32_bf16 v[4:7], a[100:103], v[128:131], v[4:7]\n"
    "v_exp_f32 v147, v147\n"
    "v_mfma_f32_16x16x32_bf16 v[36:39], a[100:103], v[136:139], v[36:39]\n"
    "ds_read_b128 a[76:79], v227 offset:16384\n"
    "v_cvt_pk_bf16_f32 v145, v146, v147\n"
    "v_mfma_f32_16x16x32_bf16 v[8:11], a[104:107], v[128:131], v[8:11]\n"
    "v_exp_f32 v152, v152\n"
    "v_mfma_f32_16x16x32_bf16 v[40:43], a[104:107], v[136:139], v[40:43]\n"
    "ds_read_b128 a[80:83], v224 offset:20480\n"
    "ds_read_b128 a[84:87], v225 offset:20480\n"
    "v_mfma_f32_16x16x32_bf16 v[12:15], a[108:111], v[128:131], v[12:15]\n"
    "v_exp_f32 v153, v153\n"
    "v_mfma_f32_16x16x32_bf16 v[44:47], a[108:111], v[136:139], v[44:47]\n"
    "ds_read_b128 a[88:91], v226 offset:20480\n"
    "v_cvt_pk_bf16_f32 v152, v152, v153\n"
    "v_mfma_f32_16x16x32_bf16 v[16:19], a[112:115], v[128:131], v[16:19]\n"
    "v_exp_f32 v154, v154\n"
    "v_mfma_f32_16x16x32_bf16 v[48:51], a[112:115], v[136:139], v[48:51]\n"
    "ds_read_b128 a[92:95], v227 offset:20480\n"
    "ds_read_b128 a[132:135], v224 offset:24576\n"
    "ds_read_b128 a[136:139], v225 offset:24576\n"
    "v_mfma_f32_16x16x32_bf16 v[20:23], a[116:119], v[128:131], v[20:23]\n"
    "v_exp_f32 v155, v155\n"
    "v_mfma_f32_16x16x32_bf16 v[52:55], a[116:119], v[136:139], v[52:55]\n"
    "v_cvt_pk_bf16_f32 v153, v154, v155\n"
    "v_mfma_f32_16x16x32_bf16 v[24:27], a[120:123], v[128:131], v[24:27]\n"
    "v_exp_f32 v148, v148\n"
    "v_mfma_f32_16x16x32_bf16 v[56:59], a[120:123], v[136:139], v[56:59]\n"
    "ds_read_b128 a[140:143], v226 offset:24576\n"
    "ds_read_b128 a[144:147], v227 offset:24576\n"
    "v_mfma_f32_16x16x32_bf16 v[28:31], a[124:127], v[128:131], v[28:31]\n"
    "v_exp_f32 v149, v149\n"
    "v_mfma_f32_16x16x32_bf16 v[60:63], a[124:127], v[136:139], v[60:63]\n"
    "v_cvt_pk_bf16_f32 v146, v148, v149\n"
    "v_mfma_f32_16x16x32_bf16 v[208:211], a[128:131], v[128:131], v[208:211]\n"
    "v_exp_f32 v150, v150\n"
    "v_mfma_f32_16x16x32_bf16 v[212:215], a[128:131], v[136:139], v[212:215]\n"
    "s_waitcnt lgkmcnt(8)\n"
    "v_mfma_f32_16x16x32_bf16 v[128:131], a[64:67], a[0:3], v[192:195]\n"
    "v_exp_f32 v151, v151\n"
    "v_mfma_f32_16x16x32_bf16 v[136:139], a[64:67], a[16:19], v[196:199]\n"
    "v_cvt_pk_bf16_f32 v147, v150, v151\n"
    "v_mfma_f32_16x16x32_bf16 v[128:131], a[68:71], a[4:7], v[128:131]\n"
    "v_exp_f32 v156, v156\n"
    "v_mfma_f32_16x16x32_bf16 v[136:139], a[68:71], a[20:23], v[136:139]\n"
    "ds_read_b128 a[148:151], v224 offset:28672\n"
    "ds_read_b128 a[152:155], v225 offset:28672\n"
    "v_mfma_f32_16x16x32_bf16 v[128:131], a[72:75], a[8:11], v[128:131]\n"
    "v_exp_f32 v157, v157\n"
    "v_mfma_f32_16x16x32_bf16 v[136:139], a[72:75], a[24:27], v[136:139]\n"
    "s_waitcnt lgkmcnt(6)\n"
    "v_cvt_pk_bf16_f32 v154, v156, v157\n"
    "v_mfma_f32_16x16x32_bf16 v[128:131], a[76:79], a[12:15], v[128:131]\n"
    "v_exp_f32 v158, v158\n"
    "v_mfma_f32_16x16x32_bf16 v[136:139], a[76:79], a[28:31], v[136:139]\n"
    "ds_read_b128 a[156:159], v226 offset:28672\n"
    "ds_read_b128 a[160:163], v227 offset:28672\n"
    "v_mfma_f32_16x16x32_bf16 v[132:135], a[80:83], a[0:3], v[192:195]\n"
    "v_exp_f32 v159, v159\n"
    "v_mfma_f32_16x16x32_bf16 v[140:143], a[80:83], a[16:19], v[196:199]\n"
    "v_cvt_pk_bf16_f32 v155, v158, v159\n"
    "v_mfma_f32_16x16x32_bf16 v[132:135], a[84:87], a[4:7], v[132:135]\n"
    "v_mfma_f32_16x16x32_bf16 v[140:143], a[84:87], a[20:23], v[140:143]\n"
    "v_mfma_f32_16x16x32_bf16 v[132:135], a[88:91], a[8:11], v[132:135]\n"
    "v_exp_f32 v128, v128\n"
    "v_mfma_f32_16x16x32_bf16 v[140:143], a[88:91], a[24:27], v[140:143]\n"
    "s_waitcnt lgkmcnt(0)\n"
    "v_mfma_f32_16x16x32_bf16 v[132:135], a[92:95], a[12:15], v[132:135]\n"
    "v_exp_f32 v129, v129\n"
    "v_mfma_f32_16x16x32_bf16 v[140:143], a[92:95], a[28:31], v[140:143]\n"
    "v_cvt_pk_bf16_f32 v128, v128, v129\n"
    "v_mfma_f32_16x16x32_bf16 v[64:67], a[96:99], v[144:147], v[64:67]\n"
    "v_exp_f32 v130, v130\n"
    "v_mfma_f32_16x16x32_bf16 v[96:99], a[96:99], v[152:155], v[96:99]\n"
    "v_mfma_f32_16x16x32_bf16 v[68:71], a[100:103], v[144:147], v[68:71]\n"
    "v_exp_f32 v131, v131\n"
    "v_mfma_f32_16x16x32_bf16 v[100:103], a[100:103], v[152:155], v[100:103]\n"
    "ds_read_b64_tr_b16 a[96:97], v228 offset:16384\n"
    "v_cvt_pk_bf16_f32 v129, v130, v131\n"
    "v_mfma_f32_16x16x32_bf16 v[72:75], a[104:107], v[144:147], v[72:75]\n"
    "v_exp_f32 v136, v136\n"
    "v_mfma_f32_16x16x32_bf16 v[104:107], a[104:107], v[152:155], v[104:107]\n"
    "ds_read_b64_tr_b16 a[98:99], v228 offset:20480\n"
    "ds_read_b64_tr_b16 a[100:101], v229 offset:16384\n"
    "v_mfma_f32_16x16x32_bf16 v[76:79], a[108:111], v[144:147], v[76:79]\n"
    "v_exp_f32 v137, v137\n"
    "v_mfma_f32_16x16x32_bf16 v[108:111], a[108:111], v[152:155], v[108:111]\n"
    "ds_read_b64_tr_b16 a[102:103], v229 offset:20480\n"
    "v_cvt_pk_bf16_f32 v136, v136, v137\n"
    "v_mfma_f32_16x16x32_bf16 v[80:83], a[112:115], v[144:147], v[80:83]\n"
    "v_exp_f32 v138, v138\n"
    "v_mfma_f32_16x16x32_bf16 v[112:115], a[112:115], v[152:155], v[112:115]\n"
    "ds_read_b64_tr_b16 a[104:105], v230 offset:16384\n"
    "ds_read_b64_tr_b16 a[106:107], v230 offset:20480\n"
    "v_mfma_f32_16x16x32_bf16 v[84:87], a[116:119], v[144:147], v[84:87]\n"
    "v_exp_f32 v139, v139\n"
    "v_mfma_f32_16x16x32_bf16 v[116:119], a[116:119], v[152:155], v[116:119]\n"
    "ds_read_b64_tr_b16 a[108:109], v231 offset:16384\n"
    "v_cvt_pk_bf16_f32 v137, v138, v139\n"
    "v_mfma_f32_16x16x32_bf16 v[88:91], a[120:123], v[144:147], v[88:91]\n"
    "v_exp_f32 v132, v132\n"
    "v_mfma_f32_16x16x32_bf16 v[120:123], a[120:123], v[152:155], v[120:123]\n"
    "ds_read_b64_tr_b16 a[110:111], v231 offset:20480\n"
    "ds_read_b64_tr_b16 a[112:113], v232 offset:16384\n"
    "v_mfma_f32_16x16x32_bf16 v[92:95], a[124:127], v[144:147], v[92:95]\n"
    "v_exp_f32 v133, v133\n"
    "v_mfma_f32_16x16x32_bf16 v[124:127], a[124:127], v[152:155], v[124:127]\n"
    "ds_read_b64_tr_b16 a[114:115], v232 offset:20480\n"
    "v_cvt_pk_bf16_f32 v130, v132, v133\n"
    "v_mfma_f32_16x16x32_bf16 v[216:219], a[128:131], v[144:147], v[216:219]\n"
    "v_exp_f32 v134, v134\n"
    "v_mfma_f32_16x16x32_bf16 v[220:223], a[128:131], v[152:155], v[220:223]\n"
    "ds_read_b64_tr_b16 a[116:117], v233 offset:16384\n"
    "ds_read_b64_tr_b16 a[118:119], v233 offset:20480\n"
    "v_mfma_f32_16x16x32_bf16 v[160:163], a[132:135], a[0:3], v[192:195]\n"
    "v_exp_f32 v135, v135\n"
    "v_mfma_f32_16x16x32_bf16 v[168:171], a[132:135], a[16:19], v[196:199]\n"
    "ds_read_b64_tr_b16 a[120:121], v234 offset:16384\n"
    "v_cvt_pk_bf16_f32 v131, v134, v135\n"
    "v_mfma_f32_16x16x32_bf16 v[160:163], a[136:139], a[4:7], v[160:163]\n"
    "v_exp_f32 v140, v140\n"
    "v_mfma_f32_16x16x32_bf16 v[168:171], a[136:139], a[20:23], v[168:171]\n"
    "ds_read_b64_tr_b16 a[122:123], v234 offset:20480\n"
    "ds_read_b64_tr_b16 a[124:125], v235 offset:16384\n"
    "v_mfma_f32_16x16x32_bf16 v[160:163], a[140:143], a[8:11], v[160:163]\n"
    "v_exp_f32 v141, v141\n"
    "v_mfma_f32_16x16x32_bf16 v[168:171], a[140:143], a[24:27], v[168:171]\n"
    "ds_read_b64_tr_b16 a[126:127], v235 offset:20480\n"
    "v_cvt_pk_bf16_f32 v138, v140, v141\n"
    "v_mfma_f32_16x16x32_bf16 v[160:163], a[144:147], a[12:15], v[160:163]\n"
    "v_exp_f32 v142, v142\n"
    "v_mfma_f32_16x16x32_bf16 v[168:171], a[144:147], a[28:31], v[168:171]\n"
    "ds_read_b64_tr_b16 a[164:165], v228 offset:24576\n"
    "ds_read_b64_tr_b16 a[166:167], v228 offset:28672\n"
    "v_mfma_f32_16x16x32_bf16 v[164:167], a[148:151], a[0:3], v[192:195]\n"
    "v_exp_f32 v143, v143\n"
    "v_mfma_f32_16x16x32_bf16 v[172:175], a[148:151], a[16:19], v[196:199]\n"
    "ds_read_b64_tr_b16 a[168:169], v229 offset:24576\n"
    "v_cvt_pk_bf16_f32 v139, v142, v143\n"
    "v_mfma_f32_16x16x32_bf16 v[164:167], a[152:155], a[4:7], v[164:167]\n"
    "ds_read_b64_tr_b16 a[170:171], v229 offset:28672\n"
    "ds_read_b64_tr_b16 a[172:173], v230 offset:24576\n"
    "v_mfma_f32_16x16x32_bf16 v[172:175], a[152:155], a[20:23], v[172:175]\n"
    "ds_read_b64_tr_b16 a[174:175], v230 offset:28672\n"
    "ds_read_b64_tr_b16 a[176:177], v231 offset:24576\n"
    "v_mfma_f32_16x16x32_bf16 v[164:167], a[156:159], a[8:11], v[164:167]\n"
    "v_exp_f32 v160, v160\n"
    "v_mfma_f32_16x16x32_bf16 v[172:175], a[156:159], a[24:27], v[172:175]\n"
    "ds_read_b64_tr_b16 a[178:179], v231 offset:28672\n"
    "ds_read_b64_tr_b16 a[180:181], v232 offset:24576\n"
    "v_mfma_f32_16x16x32_bf16 v[164:167], a[160:163], a[12:15], v[164:167]\n"
    "v_exp_f32 v161, v161\n"
    "v_mfma_f32_16x16x32_bf16 v[172:175], a[160:163], a[28:31], v[172:175]\n"
    "s_waitcnt lgkmcnt(9)\n"
    "v_cvt_pk_bf16_f32 v160, v160, v161\n"
    "v_mfma_f32_16x16x32_bf16 v[0:3], a[96:99], v[128:131], v[0:3]\n"
    "v_exp_f32 v162, v162\n"
    "v_mfma_f32_16x16x32_bf16 v[32:35], a[96:99], v[136:139], v[32:35]\n"
    "ds_read_b64_tr_b16 a[182:183], v232 offset:28672\n"
    "ds_read_b64_tr_b16 a[184:185], v233 offset:24576\n"
    "v_mfma_f32_16x16x32_bf16 v[4:7], a[100:103], v[128:131], v[4:7]\n"
    "v_exp_f32 v163, v163\n"
    "v_mfma_f32_16x16x32_bf16 v[36:39], a[100:103], v[136:139], v[36:39]\n"
    "ds_read_b64_tr_b16 a[186:187], v233 offset:28672\n"
    "v_cvt_pk_bf16_f32 v161, v162, v163\n"
    "v_mfma_f32_16x16x32_bf16 v[8:11], a[104:107], v[128:131], v[8:11]\n"
    "v_exp_f32 v168, v168\n"
    "v_mfma_f32_16x16x32_bf16 v[40:43], a[104:107], v[136:139], v[40:43]\n"
    "ds_read_b64_tr_b16 a[188:189], v234 offset:24576\n"
    "ds_read_b64_tr_b16 a[190:191], v234 offset:28672\n"
    "v_mfma_f32_16x16x32_bf16 v[12:15], a[108:111], v[128:131], v[12:15]\n"
    "v_exp_f32 v169, v169\n"
    "v_mfma_f32_16x16x32_bf16 v[44:47], a[108:111], v[136:139], v[44:47]\n"
    "ds_read_b64_tr_b16 a[192:193], v235 offset:24576\n"
    "v_cvt_pk_bf16_f32 v168, v168, v169\n"
    "v_mfma_f32_16x16x32_bf16 v[16:19], a[112:115], v[128:131], v[16:19]\n"
    "v_exp_f32 v170, v170\n"
    "v_mfma_f32_16x16x32_bf16 v[48:51], a[112:115], v[136:139], v[48:51]\n"
    "ds_read_b64_tr_b16 a[194:195], v235 offset:28672\n"
    "s_lshl_b32 s56, %[wid], 14\n"
    "s_add_u32 s56, s56, 98304\n"
    "v_mfma_f32_16x16x32_bf16 v[20:23], a[116:119], v[128:131], v[20:23]\n"
    "v_exp_f32 v171, v171\n"
    "v_mfma_f32_16x16x32_bf16 v[52:55], a[116:119], v[136:139], v[52:55]\n"
    "v_cvt_pk_bf16_f32 v169, v170, v171\n"
    "v_lshrrev_b32 v245, 5, v254\n"
    "v_mfma_f32_16x16x32_bf16 v[24:27], a[120:123], v[128:131], v[24:27]\n"
    "v_exp_f32 v164, v164\n"
    "v_mfma_f32_16x16x32_bf16 v[56:59], a[120:123], v[136:139], v[56:59]\n"
    "s_cmp_eq_u32 %[wid], 0\n"
    "s_cselect_b32 s56, 16384, s56\n"
    "s_cmp_eq_u32 %[wid], 1\n"
    "s_cselect_b32 s56, 81920, s56\n"
    "v_mfma_f32_16x16x32_bf16 v[28:31], a[124:127], v[128:131], v[28:31]\n"
    "v_exp_f32 v165, v165\n"
    "v_mfma_f32_16x16x32_bf16 v[60:63], a[124:127], v[136:139], v[60:63]\n"
    "v_cvt_pk_bf16_f32 v162, v164, v165\n"
    "v_bfe_u32 v246, v254, 4, 1\n"
    "v_mfma_f32_16x16x32_bf16 v[208:211], a[128:131], v[128:131], v[208:211]\n"
    "v_exp_f32 v166, v166\n"
    "v_mfma_f32_16x16x32_bf16 v[212:215], a[128:131], v[136:139], v[212:215]\n"
    "s_waitcnt lgkmcnt(0)\n"
    "v_lshl_add_u32 v245, v246, 1, v245\n"
    "v_mfma_f32_16x16x32_bf16 v[144:147], a[64:67], a[32:35], v[200:203]\n"
    "v_exp_f32 v167, v167\n"
    "v_mfma_f32_16x16x32_bf16 v[152:155], a[64:67], a[48:51], v[204:207]\n"
    "v_cvt_pk_bf16_f32 v163, v166, v167\n"
    "v_and_b32 v246, 7, v254\n"
    "v_mfma_f32_16x16x32_bf16 v[144:147], a[68:71], a[36:39], v[144:147]\n"
    "v_exp_f32 v172, v172\n"
    "v_mfma_f32_16x16x32_bf16 v[152:155], a[68:71], a[52:55], v[152:155]\n"
    "v_xor_b32 v245, v245, v246\n"
    "v_and_b32 v246, 15, v254\n"
    "v_mfma_f32_16x16x32_bf16 v[144:147], a[72:75], a[40:43], v[144:147]\n"
    "v_exp_f32 v173, v173\n"
    "v_mfma_f32_16x16x32_bf16 v[152:155], a[72:75], a[56:59], v[152:155]\n"
    "v_cvt_pk_bf16_f32 v170, v172, v173\n"
    "v_lshlrev_b32 v246, 8, v246\n"
    "v_mfma_f32_16x16x32_bf16 v[144:147], a[76:79], a[44:47], v[144:147]\n"
    "v_exp_f32 v174, v174\n"
    "v_mfma_f32_16x16x32_bf16 v[152:155], a[76:79], a[60:63], v[152:155]\n"
    "v_lshl_add_u32 v245, v245, 4, v246\n"
    "v_add_u32 v224, s56, v245\n"
    "v_mfma_f32_16x16x32_bf16 v[148:151], a[80:83], a[32:35], v[200:203]\n"
    "v_exp_f32 v175, v175\n"
    "v_mfma_f32_16x16x32_bf16 v[156:159], a[80:83], a[48:51], v[204:207]\n"
    "v_cvt_pk_bf16_f32 v171, v174, v175\n"
    "v_xor_b32 v225, 64, v224\n"
    "v_mfma_f32_16x16x32_bf16 v[148:151], a[84:87], a[36:39], v[148:151]\n"
    "v_add_u32 v240, s56, v236\n"
    "v_add_u32 v241, s56, v237\n"
    "v_mfma_f32_16x16x32_bf16 v[156:159], a[84:87], a[52:55], v[156:159]\n"
    "v_lshlrev_b32 v248, 4, v254\n"
    "v_mfma_f32_16x16x32_bf16 v[148:151], a[88:91], a[40:43], v[148:151]\n"
    "v_exp_f32 v144, v144\n"
    "v_mfma_f32_16x16x32_bf16 v[156:159], a[88:91], a[56:59], v[156:159]\n"
    "v_mfma_f32_16x16x32_bf16 v[148:151], a[92:95], a[44:47], v[148:151]\n"
    "v_exp_f32 v145, v145\n"
    "v_mfma_f32_16x16x32_bf16 v[156:159], a[92:95], a[60:63], v[156:159]\n"
    "v_cvt_pk_bf16_f32 v144, v144, v145\n"
    "v_mfma_f32_16x16x32_bf16 v[208:211], a[128:131], v[160:163], v[208:211]\n"
    "v_exp_f32 v146, v146\n"
    "v_mfma_f32_16x16x32_bf16 v[0:3], a[164:167], v[160:163], v[0:3]\n"
    "v_mfma_f32_16x16x32_bf16 v[4:7], a[168:171], v[160:163], v[4:7]\n"
    "v_exp_f32 v147, v147\n"
    "v_mfma_f32_16x16x32_bf16 v[8:11], a[172:175], v[160:163], v[8:11]\n"
    "v_cvt_pk_bf16_f32 v145, v146, v147\n"
    "v_mfma_f32_16x16x32_bf16 v[12:15], a[176:179], v[160:163], v[12:15]\n"
    "v_exp_f32 v152, v152\n"
    "v_mfma_f32_16x16x32_bf16 v[16:19], a[180:183], v[160:163], v[16:19]\n"
    "v_mfma_f32_16x16x32_bf16 v[20:23], a[184:187], v[160:163], v[20:23]\n"
    "v_exp_f32 v153, v153\n"
    "v_mfma_f32_16x16x32_bf16 v[24:27], a[188:191], v[160:163], v[24:27]\n"
    "v_cvt_pk_bf16_f32 v152, v152, v153\n"
    "v_rcp_f32 v250, v208\n"
    "v_mfma_f32_16x16x32_bf16 v[28:31], a[192:195], v[160:163], v[28:31]\n"
    "v_exp_f32 v154, v154\n"
    "v_pk_mul_f32 v[0:1], v[0:1], v[250:251] op_sel_hi:[1,0]\n"
    "v_pk_mul_f32 v[2:3], v[2:3], v[250:251] op_sel_hi:[1,0]\n"
    "v_pk_mul_f32 v[4:5], v[4:5], v[250:251] op_sel_hi:[1,0]\n"
    "v_pk_mul_f32 v[6:7], v[6:7], v[250:251] op_sel_hi:[1,0]\n"
    "v_cvt_pk_bf16_f32 v128, v0, v1\n"
    "v_cvt_pk_bf16_f32 v130, v4, v5\n"
    "v_cvt_pk_bf16_f32 v129, v2, v3\n"
    "v_cvt_pk_bf16_f32 v131, v6, v7\n"
    "v_pk_mul_f32 v[8:9], v[8:9], v[250:251] op_sel_hi:[1,0]\n"
    "v_pk_mul_f32 v[10:11], v[10:11], v[250:251] op_sel_hi:[1,0]\n"
    "v_permlane16_swap_b32 v128, v130\n"
    "v_permlane16_swap_b32 v129, v131\n"
    "v_pk_mul_f32 v[12:13], v[12:13], v[250:251] op_sel_hi:[1,0]\n"
    "v_pk_mul_f32 v[14:15], v[14:15], v[250:251] op_sel_hi:[1,0]\n"
    "v_cvt_pk_bf16_f32 v132, v8, v9\n"
    "v_cvt_pk_bf16_f32 v134, v12, v13\n"
    "v_cvt_pk_bf16_f32 v133, v10, v11\n"
    "v_cvt_pk_bf16_f32 v135, v14, v15\n"
    "ds_write_b128 v224, v[128:131] offset:0\n"
    "v_pk_mul_f32 v[16:17], v[16:17], v[250:251] op_sel_hi:[1,0]\n"
    "v_pk_mul_f32 v[18:19], v[18:19], v[250:251] op_sel_hi:[1,0]\n"
    "v_permlane16_swap_b32 v132, v134\n"
    "v_permlane16_swap_b32 v133, v135\n"
    "v_pk_mul_f32 v[20:21], v[20:21], v[250:251] op_sel_hi:[1,0]\n"
    "v_pk_mul_f32 v[22:23], v[22:23], v[250:251] op_sel_hi:[1,0]\n"
    "v_cvt_pk_bf16_f32 v136, v16, v17\n"
    "v_cvt_pk_bf16_f32 v138, v20, v21\n"
    "v_cvt_pk_bf16_f32 v137, v18, v19\n"
    "v_cvt_pk_bf16_f32 v139, v22, v23\n"
    "ds_write_b128 v225, v[132:135] offset:0\n"
    "v_pk_mul_f32 v[24:25], v[24:25], v[250:251] op_sel_hi:[1,0]\n"
    "v_pk_mul_f32 v[26:27], v[26:27], v[250:251] op_sel_hi:[1,0]\n"
    "v_permlane16_swap_b32 v136, v138\n"
    "v_permlane16_swap_b32 v137, v139\n"
    "v_pk_mul_f32 v[28:29], v[28:29], v[250:251] op_sel_hi:[1,0]\n"
    "v_pk_mul_f32 v[30:31], v[30:31], v[250:251] op_sel_hi:[1,0]\n"
    "v_cvt_pk_bf16_f32 v140, v24, v25\n"
    "v_cvt_pk_bf16_f32 v142, v28, v29\n"
    "v_cvt_pk_bf16_f32 v141, v26, v27\n"
    "v_cvt_pk_bf16_f32 v143, v30, v31\n"
    "ds_write_b128 v224, v[136:139] offset:128\n"
    "v_permlane16_swap_b32 v140, v142\n"
    "v_permlane16_swap_b32 v141, v143\n"
    "ds_write_b128 v225, v[140:143] offset:128\n"
    "ds_read_b128 v[128:131], v240 offset:0\n"
    "ds_read_b128 v[132:135], v241 offset:1024\n"
    "ds_read_b128 v[136:139], v240 offset:2048\n"
    "ds_read_b128 v[140:143], v241 offset:3072\n"
    "v_mfma_f32_16x16x32_bf16 v[212:215], a[128:131], v[168:171], v[212:215]\n"
    "v_mfma_f32_16x16x32_bf16 v[32:35], a[164:167], v[168:171], v[32:35]\n"
    "v_exp_f32 v155, v155\n"
    "v_mfma_f32_16x16x32_bf16 v[36:39], a[168:171], v[168:171], v[36:39]\n"
    "s_waitcnt lgkmcnt(3)\n"
    "buffer_store_dwordx4 v[128:131], v248, s[48:51], s58 offen offset:0 sc1\n"
    "v_cvt_pk_bf16_f32 v153, v154, v155\n"
    "v_mfma_f32_16x16x32_bf16 v[40:43], a[172:175], v[168:171], v[40:43]\n"
    "v_exp_f32 v148, v148\n"
    "v_mfma_f32_16x16x32_bf16 v[44:47], a[176:179], v[168:171], v[44:47]\n"
    "s_waitcnt lgkmcnt(2)\n"
    "buffer_store_dwordx4 v[132:135], v248, s[48:51], s58 offen offset:1024 sc1\n"
    "v_mfma_f32_16x16x32_bf16 v[48:51], a[180:183], v[168:171], v[48:51]\n"
    "v_exp_f32 v149, v149\n"
    "v_mfma_f32_16x16x32_bf16 v[52:55], a[184:187], v[168:171], v[52:55]\n"
    "s_waitcnt lgkmcnt(1)\n"
    "buffer_store_dwordx4 v[136:139], v248, s[48:51], s58 offen offset:2048 sc1\n"
    "v_cvt_pk_bf16_f32 v146, v148, v149\n"
    "v_mfma_f32_16x16x32_bf16 v[56:59], a[188:191], v[168:171], v[56:59]\n"
    "v_exp_f32 v150, v150\n"
    "v_rcp_f32 v252, v212\n"
    "v_mfma_f32_16x16x32_bf16 v[60:63], a[192:195], v[168:171], v[60:63]\n"
    "s_waitcnt lgkmcnt(0)\n"
    "buffer_store_dwordx4 v[140:143], v248, s[48:51], s58 offen offset:3072 sc1\n"
    "v_pk_mul_f32 v[32:33], v[32:33], v[252:253] op_sel_hi:[1,0]\n"
    "v_pk_mul_f32 v[34:35], v[34:35], v[252:253] op_sel_hi:[1,0]\n"
    "v_pk_mul_f32 v[36:37], v[36:37], v[252:253] op_sel_hi:[1,0]\n"
    "v_pk_mul_f32 v[38:39], v[38:39], v[252:253] op_sel_hi:[1,0]\n"
    "v_cvt_pk_bf16_f32 v160, v32, v33\n"
    "v_cvt_pk_bf16_f32 v162, v36, v37\n"
    "v_cvt_pk_bf16_f32 v161, v34, v35\n"
    "v_cvt_pk_bf16_f32 v163, v38, v39\n"
    "v_pk_mul_f32 v[40:41], v[40:41], v[252:253] op_sel_hi:[1,0]\n"
    "v_pk_mul_f32 v[42:43], v[42:43], v[252:253] op_sel_hi:[1,0]\n"
    "v_permlane16_swap_b32 v160, v162\n"
    "v_permlane16_swap_b32 v161, v163\n"
    "v_pk_mul_f32 v[44:45], v[44:45], v[252:253] op_sel_hi:[1,0]\n"
    "v_pk_mul_f32 v[46:47], v[46:47], v[252:253] op_sel_hi:[1,0]\n"
    "v_cvt_pk_bf16_f32 v164, v40, v41\n"
    "v_cvt_pk_bf16_f32 v166, v44, v45\n"
    "v_cvt_pk_bf16_f32 v165, v42, v43\n"
    "v_cvt_pk_bf16_f32 v167, v46, v47\n"
    "ds_write_b128 v224, v[160:163] offset:4096\n"
    "v_pk_mul_f32 v[48:49], v[48:49], v[252:253] op_sel_hi:[1,0]\n"
    "v_pk_mul_f32 v[50:51], v[50:51], v[252:253] op_sel_hi:[1,0]\n"
    "v_permlane16_swap_b32 v164, v166\n"
    "v_permlane16_swap_b32 v165, v167\n"
    "v_pk_mul_f32 v[52:53], v[52:53], v[252:253] op_sel_hi:[1,0]\n"
    "v_pk_mul_f32 v[54:55], v[54:55], v[252:253] op_sel_hi:[1,0]\n"
    "v_cvt_pk_bf16_f32 v168, v48, v49\n"
    "v_cvt_pk_bf16_f32 v170, v52, v53\n"
    "v_cvt_pk_bf16_f32 v169, v50, v51\n"
    "v_cvt_pk_bf16_f32 v171, v54, v55\n"
    "ds_write_b128 v225, v[164:167] offset:4096\n"
    "v_pk_mul_f32 v[56:57], v[56:57], v[252:253] op_sel_hi:[1,0]\n"
    "v_pk_mul_f32 v[58:59], v[58:59], v[252:253] op_sel_hi:[1,0]\n"
    "v_permlane16_swap_b32 v168, v170\n"
    "v_permlane16_swap_b32 v169, v171\n"
    "v_pk_mul_f32 v[60:61], v[60:61], v[252:253] op_sel_hi:[1,0]\n"
    "v_pk_mul_f32 v[62:63], v[62:63], v[252:253] op_sel_hi:[1,0]\n"
    "v_cvt_pk_bf16_f32 v172, v56, v57\n"
    "v_cvt_pk_bf16_f32 v174, v60, v61\n"
    "v_cvt_pk_bf16_f32 v173, v58, v59\n"
    "v_cvt_pk_bf16_f32 v175, v62, v63\n"
    "ds_write_b128 v224, v[168:171] offset:4224\n"
    "v_permlane16_swap_b32 v172, v174\n"
    "v_permlane16_swap_b32 v173, v175\n"
    "ds_write_b128 v225, v[172:175] offset:4224\n"
    "ds_read_b128 v[160:163], v240 offset:4096\n"
    "ds_read_b128 v[164:167], v241 offset:5120\n"
    "ds_read_b128 v[168:171], v240 offset:6144\n"
    "ds_read_b128 v[172:175], v241 offset:7168\n"
    "v_mfma_f32_16x16x32_bf16 v[176:179], a[132:135], a[32:35], v[200:203]\n"
    "v_exp_f32 v151, v151\n"
    "v_mfma_f32_16x16x32_bf16 v[184:187], a[132:135], a[48:51], v[204:207]\n"
    "v_cvt_pk_bf16_f32 v147, v150, v151\n"
    "v_mfma_f32_16x16x32_bf16 v[176:179], a[136:139], a[36:39], v[176:179]\n"
    "v_exp_f32 v156, v156\n"
    "v_mfma_f32_16x16x32_bf16 v[184:187], a[136:139], a[52:55], v[184:187]\n"
    "s_waitcnt lgkmcnt(3)\n"
    "buffer_store_dwordx4 v[160:163], v248, s[48:51], s59 offen offset:0 sc1\n"
    "v_mfma_f32_16x16x32_bf16 v[176:179], a[140:143], a[40:43], v[176:179]\n"
    "v_exp_f32 v157, v157\n"
    "v_mfma_f32_16x16x32_bf16 v[184:187], a[140:143], a[56:59], v[184:187]\n"
    "v_cvt_pk_bf16_f32 v154, v156, v157\n"
    "v_mfma_f32_16x16x32_bf16 v[176:179], a[144:147], a[44:47], v[176:179]\n"
    "v_exp_f32 v158, v158\n"
    "v_mfma_f32_16x16x32_bf16 v[184:187], a[144:147], a[60:63], v[184:187]\n"
    "s_waitcnt lgkmcnt(2)\n"
    "buffer_store_dwordx4 v[164:167], v248, s[48:51], s59 offen offset:1024 sc1\n"
    "v_mfma_f32_16x16x32_bf16 v[180:183], a[148:151], a[32:35], v[200:203]\n"
    "v_exp_f32 v159, v159\n"
    "v_mfma_f32_16x16x32_bf16 v[188:191], a[148:151], a[48:51], v[204:207]\n"
    "v_cvt_pk_bf16_f32 v155, v158, v159\n"
    "v_mfma_f32_16x16x32_bf16 v[180:183], a[152:155], a[36:39], v[180:183]\n"
    "v_mfma_f32_16x16x32_bf16 v[188:191], a[152:155], a[52:55], v[188:191]\n"
    "s_waitcnt lgkmcnt(1)\n"
    "buffer_store_dwordx4 v[168:171], v248, s[48:51], s59 offen offset:2048 sc1\n"
    "v_exp_f32 v176, v176\n"
    "v_mfma_f32_16x16x32_bf16 v[180:183], a[156:159], a[40:43], v[180:183]\n"
    "v_exp_f32 v177, v177\n"
    "v_mfma_f32_16x16x32_bf16 v[188:191], a[156:159], a[56:59], v[188:191]\n"
    "v_exp_f32 v178, v178\n"
    "v_mfma_f32_16x16x32_bf16 v[180:183], a[160:163], a[44:47], v[180:183]\n"
    "v_exp_f32 v179, v179\n"
    "v_mfma_f32_16x16x32_bf16 v[188:191], a[160:163], a[60:63], v[188:191]\n"
    "s_waitcnt lgkmcnt(0)\n"
    "buffer_store_dwordx4 v[172:175], v248, s[48:51], s59 offen offset:3072 sc1\n"
    "v_exp_f32 v184, v184\n"
    "v_mfma_f32_16x16x32_bf16 v[64:67], a[96:99], v[144:147], v[64:67]\n"
    "v_exp_f32 v185, v185\n"
    "v_mfma_f32_16x16x32_bf16 v[96:99], a[96:99], v[152:155], v[96:99]\n"
    "v_exp_f32 v186, v186\n"
    "v_mfma_f32_16x16x32_bf16 v[68:71], a[100:103], v[144:147], v[68:71]\n"
    "v_exp_f32 v187, v187\n"
    "v_mfma_f32_16x16x32_bf16 v[100:103], a[100:103], v[152:155], v[100:103]\n"
    "v_exp_f32 v180, v180\n"
    "v_mfma_f32_16x16x32_bf16 v[72:75], a[104:107], v[144:147], v[72:75]\n"
    "v_exp_f32 v181, v181\n"
    "v_mfma_f32_16x16x32_bf16 v[104:107], a[104:107], v[152:155], v[104:107]\n"
    "v_exp_f32 v182, v182\n"
    "v_mfma_f32_16x16x32_bf16 v[76:79], a[108:111], v[144:147], v[76:79]\n"
    "v_exp_f32 v183, v183\n"
    "v_mfma_f32_16x16x32_bf16 v[108:111], a[108:111], v[152:155], v[108:111]\n"
    "v_exp_f32 v188, v188\n"
    "v_mfma_f32_16x16x32_bf16 v[80:83], a[112:115], v[144:147], v[80:83]\n"
    "v_exp_f32 v189, v189\n"
    "v_mfma_f32_16x16x32_bf16 v[112:115], a[112:115], v[152:155], v[112:115]\n"
    "v_exp_f32 v190, v190\n"
    "v_mfma_f32_16x16x32_bf16 v[84:87], a[116:119], v[144:147], v[84:87]\n"
    "v_exp_f32 v191, v191\n"
    "v_mfma_f32_16x16x32_bf16 v[116:119], a[116:119], v[152:155], v[116:119]\n"
    "v_cvt_pk_bf16_f32 v176, v176, v177\n"
    "v_cvt_pk_bf16_f32 v177, v178, v179\n"
    "v_mfma_f32_16x16x32_bf16 v[88:91], a[120:123], v[144:147], v[88:91]\n"
    "v_cvt_pk_bf16_f32 v184, v184, v185\n"
    "v_cvt_pk_bf16_f32 v185, v186, v187\n"
    "v_mfma_f32_16x16x32_bf16 v[120:123], a[120:123], v[152:155], v[120:123]\n"
    "v_cvt_pk_bf16_f32 v178, v180, v181\n"
    "v_cvt_pk_bf16_f32 v179, v182, v183\n"
    "v_mfma_f32_16x16x32_bf16 v[92:95], a[124:127], v[144:147], v[92:95]\n"
    "v_cvt_pk_bf16_f32 v186, v188, v189\n"
    "v_cvt_pk_bf16_f32 v187, v190, v191\n"
    "v_mfma_f32_16x16x32_bf16 v[124:127], a[124:127], v[152:155], v[124:127]\n"
    "v_mfma_f32_16x16x32_bf16 v[216:219], a[128:131], v[144:147], v[216:219]\n"
    "v_mfma_f32_16x16x32_bf16 v[220:223], a[128:131], v[152:155], v[220:223]\n"
    "v_mfma_f32_16x16x32_bf16 v[216:219], a[128:131], v[176:179], v[216:219]\n"
    "v_mfma_f32_16x16x32_bf16 v[64:67], a[164:167], v[176:179], v[64:67]\n"
    "v_mfma_f32_16x16x32_bf16 v[68:71], a[168:171], v[176:179], v[68:71]\n"
    "v_mfma_f32_16x16x32_bf16 v[72:75], a[172:175], v[176:179], v[72:75]\n"
    "v_mfma_f32_16x16x32_bf16 v[76:79], a[176:179], v[176:179], v[76:79]\n"
    "v_mfma_f32_16x16x32_bf16 v[80:83], a[180:183], v[176:179], v[80:83]\n"
    "v_mfma_f32_16x16x32_bf16 v[84:87], a[184:187], v[176:179], v[84:87]\n"
    "v_mfma_f32_16x16x32_bf16 v[88:91], a[188:191], v[176:179], v[88:91]\n"
    "v_mfma_f32_16x16x32_bf16 v[92:95], a[192:195], v[176:179], v[92:95]\n"
    "s_nop 0\n"
    "v_rcp_f32 v250, v216\n"
    "s_nop 0\n"
    "v_pk_mul_f32 v[64:65], v[64:65], v[250:251] op_sel_hi:[1,0]\n"
    "v_pk_mul_f32 v[66:67], v[66:67], v[250:251] op_sel_hi:[1,0]\n"
    "v_pk_mul_f32 v[68:69], v[68:69], v[250:251] op_sel_hi:[1,0]\n"
    "v_pk_mul_f32 v[70:71], v[70:71], v[250:251] op_sel_hi:[1,0]\n"
    "v_cvt_pk_bf16_f32 v128, v64, v65\n"
    "v_cvt_pk_bf16_f32 v130, v68, v69\n"
    "v_cvt_pk_bf16_f32 v129, v66, v67\n"
    "v_cvt_pk_bf16_f32 v131, v70, v71\n"
    "v_pk_mul_f32 v[72:73], v[72:73], v[250:251] op_sel_hi:[1,0]\n"
    "v_pk_mul_f32 v[74:75], v[74:75], v[250:251] op_sel_hi:[1,0]\n"
    "v_permlane16_swap_b32 v128, v130\n"
    "v_permlane16_swap_b32 v129, v131\n"
    "v_pk_mul_f32 v[76:77], v[76:77], v[250:251] op_sel_hi:[1,0]\n"
    "v_pk_mul_f32 v[78:79], v[78:79], v[250:251] op_sel_hi:[1,0]\n"
    "v_cvt_pk_bf16_f32 v132, v72, v73\n"
    "v_cvt_pk_bf16_f32 v134, v76, v77\n"
    "v_cvt_pk_bf16_f32 v133, v74, v75\n"
    "v_cvt_pk_bf16_f32 v135, v78, v79\n"
    "ds_write_b128 v224, v[128:131] offset:8192\n"
    "v_pk_mul_f32 v[80:81], v[80:81], v[250:251] op_sel_hi:[1,0]\n"
    "v_pk_mul_f32 v[82:83], v[82:83], v[250:251] op_sel_hi:[1,0]\n"
    "v_permlane16_swap_b32 v132, v134\n"
    "v_permlane16_swap_b32 v133, v135\n"
    "v_pk_mul_f32 v[84:85], v[84:85], v[250:251] op_sel_hi:[1,0]\n"
    "v_pk_mul_f32 v[86:87], v[86:87], v[250:251] op_sel_hi:[1,0]\n"
    "v_cvt_pk_bf16_f32 v136, v80, v81\n"
    "v_cvt_pk_bf16_f32 v138, v84, v85\n"
    "v_cvt_pk_bf16_f32 v137, v82, v83\n"
    "v_cvt_pk_bf16_f32 v139, v86, v87\n"
    "ds_write_b128 v225, v[132:135] offset:8192\n"
    "v_pk_mul_f32 v[88:89], v[88:89], v[250:251] op_sel_hi:[1,0]\n"
    "v_pk_mul_f32 v[90:91], v[90:91], v[250:251] op_sel_hi:[1,0]\n"
    "v_permlane16_swap_b32 v136, v138\n"
    "v_permlane16_swap_b32 v137, v139\n"
    "v_pk_mul_f32 v[92:93], v[92:93], v[250:251] op_sel_hi:[1,0]\n"
    "v_pk_mul_f32 v[94:95], v[94:95], v[250:251] op_sel_hi:[1,0]\n"
    "v_cvt_pk_bf16_f32 v140, v88, v89\n"
    "v_cvt_pk_bf16_f32 v142, v92, v93\n"
    "v_cvt_pk_bf16_f32 v141, v90, v91\n"
    "v_cvt_pk_bf16_f32 v143, v94, v95\n"
    "ds_write_b128 v224, v[136:139] offset:8320\n"
    "v_permlane16_swap_b32 v140, v142\n"
    "v_permlane16_swap_b32 v141, v143\n"
    "ds_write_b128 v225, v[140:143] offset:8320\n"
    "ds_read_b128 v[128:131], v240 offset:8192\n"
    "ds_read_b128 v[132:135], v241 offset:9216\n"
    "ds_read_b128 v[136:139], v240 offset:10240\n"
    "ds_read_b128 v[140:143], v241 offset:11264\n"
    "v_mfma_f32_16x16x32_bf16 v[220:223], a[128:131], v[184:187], v[220:223]\n"
    "v_mfma_f32_16x16x32_bf16 v[96:99], a[164:167], v[184:187], v[96:99]\n"
    "v_mfma_f32_16x16x32_bf16 v[100:103], a[168:171], v[184:187], v[100:103]\n"
    "s_waitcnt lgkmcnt(3)\n"
    "buffer_store_dwordx4 v[128:131], v248, s[48:51], s60 offen offset:0 sc1\n"
    "v_mfma_f32_16x16x32_bf16 v[104:107], a[172:175], v[184:187], v[104:107]\n"
    "v_mfma_f32_16x16x32_bf16 v[108:111], a[176:179], v[184:187], v[108:111]\n"
    "s_waitcnt lgkmcnt(2)\n"
    "buffer_store_dwordx4 v[132:135], v248, s[48:51], s60 offen offset:1024 sc1\n"
    "v_mfma_f32_16x16x32_bf16 v[112:115], a[180:183], v[184:187], v[112:115]\n"
    "v_mfma_f32_16x16x32_bf16 v[116:119], a[184:187], v[184:187], v[116:119]\n"
    "s_waitcnt lgkmcnt(1)\n"
    "buffer_store_dwordx4 v[136:139], v248, s[48:51], s60 offen offset:2048 sc1\n"
    "v_mfma_f32_16x16x32_bf16 v[120:123], a[188:191], v[184:187], v[120:123]\n"
    "v_mfma_f32_16x16x32_bf16 v[124:127], a[192:195], v[184:187], v[124:127]\n"
    "s_waitcnt lgkmcnt(0)\n"
    "buffer_store_dwordx4 v[140:143], v248, s[48:51], s60 offen offset:3072 sc1\n"
    "v_rcp_f32 v252, v220\n"
    "s_nop 0\n"
    "v_pk_mul_f32 v[96:97], v[96:97], v[252:253] op_sel_hi:[1,0]\n"
    "v_pk_mul_f32 v[98:99], v[98:99], v[252:253] op_sel_hi:[1,0]\n"
    "v_pk_mul_f32 v[100:101], v[100:101], v[252:253] op_sel_hi:[1,0]\n"
    "v_pk_mul_f32 v[102:103], v[102:103], v[252:253] op_sel_hi:[1,0]\n"
    "v_cvt_pk_bf16_f32 v160, v96, v97\n"
    "v_cvt_pk_bf16_f32 v162, v100, v101\n"
    "v_cvt_pk_bf16_f32 v161, v98, v99\n"
    "v_cvt_pk_bf16_f32 v163, v102, v103\n"
    "v_pk_mul_f32 v[104:105], v[104:105], v[252:253] op_sel_hi:[1,0]\n"
    "v_pk_mul_f32 v[106:107], v[106:107], v[252:253] op_sel_hi:[1,0]\n"
    "v_permlane16_swap_b32 v160, v162\n"
    "v_permlane16_swap_b32 v161, v163\n"
    "v_pk_mul_f32 v[108:109], v[108:109], v[252:253] op_sel_hi:[1,0]\n"
    "v_pk_mul_f32 v[110:111], v[110:111], v[252:253] op_sel_hi:[1,0]\n"
    "v_cvt_pk_bf16_f32 v164, v104, v105\n"
    "v_cvt_pk_bf16_f32 v166, v108, v109\n"
    "v_cvt_pk_bf16_f32 v165, v106, v107\n"
    "v_cvt_pk_bf16_f32 v167, v110, v111\n"
    "ds_write_b128 v224, v[160:163] offset:12288\n"
    "v_pk_mul_f32 v[112:113], v[112:113], v[252:253] op_sel_hi:[1,0]\n"
    "v_pk_mul_f32 v[114:115], v[114:115], v[252:253] op_sel_hi:[1,0]\n"
    "v_permlane16_swap_b32 v164, v166\n"
    "v_permlane16_swap_b32 v165, v167\n"
    "v_pk_mul_f32 v[116:117], v[116:117], v[252:253] op_sel_hi:[1,0]\n"
    "v_pk_mul_f32 v[118:119], v[118:119], v[252:253] op_sel_hi:[1,0]\n"
    "v_cvt_pk_bf16_f32 v168, v112, v113\n"
    "v_cvt_pk_bf16_f32 v170, v116, v117\n"
    "v_cvt_pk_bf16_f32 v169, v114, v115\n"
    "v_cvt_pk_bf16_f32 v171, v118, v119\n"
    "ds_write_b128 v225, v[164:167] offset:12288\n"
    "v_pk_mul_f32 v[120:121], v[120:121], v[252:253] op_sel_hi:[1,0]\n"
    "v_pk_mul_f32 v[122:123], v[122:123], v[252:253] op_sel_hi:[1,0]\n"
    "v_permlane16_swap_b32 v168, v170\n"
    "v_permlane16_swap_b32 v169, v171\n"
    "v_pk_mul_f32 v[124:125], v[124:125], v[252:253] op_sel_hi:[1,0]\n"
    "v_pk_mul_f32 v[126:127], v[126:127], v[252:253] op_sel_hi:[1,0]\n"
    "v_cvt_pk_bf16_f32 v172, v120, v121\n"
    "v_cvt_pk_bf16_f32 v174, v124, v125\n"
    "v_cvt_pk_bf16_f32 v173, v122, v123\n"
    "v_cvt_pk_bf16_f32 v175, v126, v127\n"
    "ds_write_b128 v224, v[168:171] offset:12416\n"
    "v_permlane16_swap_b32 v172, v174\n"
    "v_permlane16_swap_b32 v173, v175\n"
    "ds_write_b128 v225, v[172:175] offset:12416\n"
    "ds_read_b128 v[160:163], v240 offset:12288\n"
    "ds_read_b128 v[164:167], v241 offset:13312\n"
    "ds_read_b128 v[168:171], v240 offset:14336\n"
    "ds_read_b128 v[172:175], v241 offset:15360\n"
    "s_waitcnt lgkmcnt(3)\n"
    "buffer_store_dwordx4 v[160:163], v248, s[48:51], s61 offen offset:0 sc1\n"
    "s_waitcnt lgkmcnt(2)\n"
    "buffer_store_dwordx4 v[164:167], v248, s[48:51], s61 offen offset:1024 sc1\n"
    "s_waitcnt lgkmcnt(1)\n"
    "buffer_store_dwordx4 v[168:171], v248, s[48:51], s61 offen offset:2048 sc1\n"
    "s_waitcnt lgkmcnt(0)\n"
    "buffer_store_dwordx4 v[172:175], v248, s[48:51], s61 offen offset:3072 sc1\n"
    "v_mov_b32 v244, 0\n"
    "v_mov_b32 v245, 0\n"
    "v_pk_fma_f32 v[244:245], v[0:1], 0, v[244:245] op_sel_hi:[1,0,1]\n"
    "v_pk_fma_f32 v[244:245], v[2:3], 0, v[244:245] op_sel_hi:[1,0,1]\n"
    "v_pk_fma_f32 v[244:245], v[4:5], 0, v[244:245] op_sel_hi:[1,0,1]\n"
    "v_pk_fma_f32 v[244:245], v[6:7], 0, v[244:245] op_sel_hi:[1,0,1]\n"
    "v_pk_fma_f32 v[244:245], v[8:9], 0, v[244:245] op_sel_hi:[1,0,1]\n"
    "v_pk_fma_f32 v[244:245], v[10:11], 0, v[244:245] op_sel_hi:[1,0,1]\n"
    "v_pk_fma_f32 v[244:245], v[12:13], 0, v[244:245] op_sel_hi:[1,0,1]\n"
    "v_pk_fma_f32 v[244:245], v[14:15], 0, v[244:245] op_sel_hi:[1,0,1]\n"
    "v_pk_fma_f32 v[244:245], v[16:17], 0, v[244:245] op_sel_hi:[1,0,1]\n"
    "v_pk_fma_f32 v[244:245], v[18:19], 0, v[244:245] op_sel_hi:[1,0,1]\n"
    "v_pk_fma_f32 v[244:245], v[20:21], 0, v[244:245] op_sel_hi:[1,0,1]\n"
    "v_pk_fma_f32 v[244:245], v[22:23], 0, v[244:245] op_sel_hi:[1,0,1]\n"
    "v_pk_fma_f32 v[244:245], v[24:25], 0, v[244:245] op_sel_hi:[1,0,1]\n"
    "v_pk_fma_f32 v[244:245], v[26:27], 0, v[244:245] op_sel_hi:[1,0,1]\n"
    "v_pk_fma_f32 v[244:245], v[28:29], 0, v[244:245] op_sel_hi:[1,0,1]\n"
    "v_pk_fma_f32 v[244:245], v[30:31], 0, v[244:245] op_sel_hi:[1,0,1]\n"
    "v_pk_fma_f32 v[244:245], v[32:33], 0, v[244:245] op_sel_hi:[1,0,1]\n"
    "v_pk_fma_f32 v[244:245], v[34:35], 0, v[244:245] op_sel_hi:[1,0,1]\n"
    "v_pk_fma_f32 v[244:245], v[36:37], 0, v[244:245] op_sel_hi:[1,0,1]\n"
    "v_pk_fma_f32 v[244:245], v[38:39], 0, v[244:245] op_sel_hi:[1,0,1]\n"
    "v_pk_fma_f32 v[244:245], v[40:41], 0, v[244:245] op_sel_hi:[1,0,1]\n"
    "v_pk_fma_f32 v[244:245], v[42:43], 0, v[244:245] op_sel_hi:[1,0,1]\n"
    "v_pk_fma_f32 v[244:245], v[44:45], 0, v[244:245] op_sel_hi:[1,0,1]\n"
    "v_pk_fma_f32 v[244:245], v[46:47], 0, v[244:245] op_sel_hi:[1,0,1]\n"
    "v_pk_fma_f32 v[244:245], v[48:49], 0, v[244:245] op_sel_hi:[1,0,1]\n"
    "v_pk_fma_f32 v[244:245], v[50:51], 0, v[244:245] op_sel_hi:[1,0,1]\n"
    "v_pk_fma_f32 v[244:245], v[52:53], 0, v[244:245] op_sel_hi:[1,0,1]\n"
    "v_pk_fma_f32 v[244:245], v[54:55], 0, v[244:245] op_sel_hi:[1,0,1]\n"
    "v_pk_fma_f32 v[244:245], v[56:57], 0, v[244:245] op_sel_hi:[1,0,1]\n"
    "v_pk_fma_f32 v[244:245], v[58:59], 0, v[244:245] op_sel_hi:[1,0,1]\n"
    "v_pk_fma_f32 v[244:245], v[60:61], 0, v[244:245] op_sel_hi:[1,0,1]\n"
    "v_pk_fma_f32 v[244:245], v[62:63], 0, v[244:245] op_sel_hi:[1,0,1]\n"
    "v_pk_fma_f32 v[244:245], v[64:65], 0, v[244:245] op_sel_hi:[1,0,1]\n"
    "v_pk_fma_f32 v[244:245], v[66:67], 0, v[244:245] op_sel_hi:[1,0,1]\n"
    "v_pk_fma_f32 v[244:245], v[68:69], 0, v[244:245] op_sel_hi:[1,0,1]\n"
    "v_pk_fma_f32 v[244:245], v[70:71], 0, v[244:245] op_sel_hi:[1,0,1]\n"
    "v_pk_fma_f32 v[244:245], v[72:73], 0, v[244:245] op_sel_hi:[1,0,1]\n"
    "v_pk_fma_f32 v[244:245], v[74:75], 0, v[244:245] op_sel_hi:[1,0,1]\n"
    "v_pk_fma_f32 v[244:245], v[76:77], 0, v[244:245] op_sel_hi:[1,0,1]\n"
    "v_pk_fma_f32 v[244:245], v[78:79], 0, v[244:245] op_sel_hi:[1,0,1]\n"
    "v_pk_fma_f32 v[244:245], v[80:81], 0, v[244:245] op_sel_hi:[1,0,1]\n"
    "v_pk_fma_f32 v[244:245], v[82:83], 0, v[244:245] op_sel_hi:[1,0,1]\n"
    "v_pk_fma_f32 v[244:245], v[84:85], 0, v[244:245] op_sel_hi:[1,0,1]\n"
    "v_pk_fma_f32 v[244:245], v[86:87], 0, v[244:245] op_sel_hi:[1,0,1]\n"
    "v_pk_fma_f32 v[244:245], v[88:89], 0, v[244:245] op_sel_hi:[1,0,1]\n"
    "v_pk_fma_f32 v[244:245], v[90:91], 0, v[244:245] op_sel_hi:[1,0,1]\n"
    "v_pk_fma_f32 v[244:245], v[92:93], 0, v[244:245] op_sel_hi:[1,0,1]\n"
    "v_pk_fma_f32 v[244:245], v[94:95], 0, v[244:245] op_sel_hi:[1,0,1]\n"
    "v_pk_fma_f32 v[244:245], v[96:97], 0, v[244:245] op_sel_hi:[1,0,1]\n"
    "v_pk_fma_f32 v[244:245], v[98:99], 0, v[244:245] op_sel_hi:[1,0,1]\n"
    "v_pk_fma_f32 v[244:245], v[100:101], 0, v[244:245] op_sel_hi:[1,0,1]\n"
    "v_pk_fma_f32 v[244:245], v[102:103], 0, v[244:245] op_sel_hi:[1,0,1]\n"
    "v_pk_fma_f32 v[244:245], v[104:105], 0, v[244:245] op_sel_hi:[1,0,1]\n"
    "v_pk_fma_f32 v[244:245], v[106:107], 0, v[244:245] op_sel_hi:[1,0,1]\n"
    "v_pk_fma_f32 v[244:245], v[108:109], 0, v[244:245] op_sel_hi:[1,0,1]\n"
    "v_pk_fma_f32 v[244:245], v[110:111], 0, v[244:245] op_sel_hi:[1,0,1]\n"
    "v_pk_fma_f32 v[244:245], v[112:113], 0, v[244:245] op_sel_hi:[1,0,1]\n"
    "v_pk_fma_f32 v[244:245], v[114:115], 0, v[244:245] op_sel_hi:[1,0,1]\n"
    "v_pk_fma_f32 v[244:245], v[116:117], 0, v[244:245] op_sel_hi:[1,0,1]\n"
    "v_pk_fma_f32 v[244:245], v[118:119], 0, v[244:245] op_sel_hi:[1,0,1]\n"
    "v_pk_fma_f32 v[244:245], v[120:121], 0, v[244:245] op_sel_hi:[1,0,1]\n"
    "v_pk_fma_f32 v[244:245], v[122:123], 0, v[244:245] op_sel_hi:[1,0,1]\n"
    "v_pk_fma_f32 v[244:245], v[124:125], 0, v[244:245] op_sel_hi:[1,0,1]\n"
    "v_pk_fma_f32 v[244:245], v[126:127], 0, v[244:245] op_sel_hi:[1,0,1]\n"
    "s_mov_b32 s57, 0x7b800000\n"
    "v_add_f32 v244, v244, v245\n"
    "v_add_f32 v246, v208, v212\n"
    "v_add_f32 v245, v216, v220\n"
    "v_add_f32 v246, v246, v245\n"
    "v_add_f32 v246, v246, v244\n"
    "v_cmp_nle_f32 s[66:67], v246, s57\n"
    "s_nop 0\n"
    "s_cmp_lg_u64 s[66:67], 0\n"
    "s_cselect_b32 %[flag], 1, 0\n"
    ".Lattn_end:\n"
    : [flag] "=&s"(flag)
    : [ka] "s"(__builtin_amdgcn_kernarg_segment_ptr()), [bid] "s"(bid), [wid] "s"(wid), [lds] "s"((unsigned)(size_t)lds)
    : "memory", "vcc", "scc", CLOB_V, CLOB_A, CLOB_S);
  if (flag) {
    unsigned lane;
    asm volatile("s_waitcnt vmcnt(0)\n\tv_mbcnt_lo_u32_b32 %0, -1, 0\n\tv_mbcnt_hi_u32_b32 %0, -1, %0" : "=v"(lane) :: "memory");
    const unsigned head = (bid & 7u) * 32u + (bid >> 3);
    slow_rows((const unsigned*)q + (size_t)head * 16384 + wid * 1024, (const unsigned*)k + (size_t)head * 65536, (const unsigned*)v + (size_t)head * 65536,
              (unsigned*)o + (size_t)head * 16384 + wid * 1024, lane);
  }
}

extern "C" void kernel_launch(void* const* d_in, const int* in_sizes, int n_in, void* d_out, int out_size, void* d_ws, size_t ws_size,
                              hipStream_t stream) {
  attn_fwd<<<256, 256, 0, stream>>>(d_in[0], d_in[1], d_in[2], d_out);
}
```
